```python
import torch

def _softmax(x, axis=-1):
    return torch.softmax(x, dim=axis)

def _gelu(x):
    return torch.nn.functional.gelu(x, approximate='tanh')

def _silu(x):
    return torch.nn.functional.silu(x)

def _sigmoid(x):
    return torch.sigmoid(x)

def _layernorm(x, axis=-1, eps=1e-05):
    m = x.mean(dim=axis, keepdim=True)
    v = x.var(dim=axis, keepdim=True, unbiased=False)
    return (x - m) / torch.sqrt(v + eps)

def _scatter_add(tgt, idx, src, axis=0):
    return tgt.clone().index_add_(axis, idx, src)

def _segment_sum(src, seg, n):
    out = torch.zeros((n,) + src.shape[1:], dtype=src.dtype)
    return out.index_add_(0, seg, src)

def _pad(x, pw):
    flat = tuple((v for pair in reversed(pw) for v in pair))
    return torch.nn.functional.pad(x, flat)
kernel_name = 'adaLN_cond__spectral_fnet__moe_arch_mod__prompt'

def setup_inputs():

    _w = lambda a: (lambda t: t.to(torch.bfloat16) if t.is_floating_point() else t)(torch.as_tensor(a))

    def _A_setup():
        B, S, D = (1, 2048, 512)
        ns = torch.arange(S)[:, None] * torch.arange(S)[None, :]
        nd = torch.arange(D)[:, None] * torch.arange(D)[None, :]
        Ws_re = torch.cos(-2.0 * torch.pi * ns / S).to(float)
        Ws_im = torch.sin(-2.0 * torch.pi * ns / S).to(float)
        Wd_re = torch.cos(-2.0 * torch.pi * nd / D).to(float)
        Wd_im = torch.sin(-2.0 * torch.pi * nd / D).to(float)
        return {'x': _w((lambda *s: torch.randn(*s, dtype=torch.bfloat16) * 0.05)(B, S, D)), 'dft_seq_re': _w(Ws_re), 'dft_seq_im': _w(Ws_im), 'dft_hid_re': _w(Wd_re), 'dft_hid_im': _w(Wd_im)}
    _A_in = _A_setup()

    def _B_setup():
        B, S, D, F, C = (1, 2048, 512, 1792, 2)
        return {'x': _w((lambda *s: torch.randn(*s, dtype=torch.bfloat16) * 0.05)(B, S, D)), 'w_r': _w((lambda *s: torch.randn(*s, dtype=torch.bfloat16) * 3.2)(D)), 'w_gate': _w((lambda *s: torch.randn(*s, dtype=torch.bfloat16) * 0.8)(D, F)), 'w_up': _w((lambda *s: torch.randn(*s, dtype=torch.bfloat16) * 0.8)(D, F)), 'w_down': _w((lambda *s: torch.randn(*s, dtype=torch.bfloat16) * 0.8)(F, D))}
    _B_in = _B_setup()
    return {'A_x': _A_in['x'], 'A_dft_seq_re': _A_in['dft_seq_re'], 'A_dft_seq_im': _A_in['dft_seq_im'], 'A_dft_hid_re': _A_in['dft_hid_re'], 'A_dft_hid_im': _A_in['dft_hid_im'], 'B_x': _B_in['x'], 'B_w_r': _B_in['w_r'], 'B_w_gate': _B_in['w_gate'], 'B_w_up': _B_in['w_up'], 'B_w_down': _B_in['w_down'], 'c': _w((lambda *s: torch.randn(*s, dtype=torch.bfloat16) * 0.1)(1, 512) * 8.0), 'w_mod': _w((lambda *s: torch.randn(*s, dtype=torch.bfloat16) * 0.1)(512, 6 * 512) * 512 ** (-0.5) * 8.0)}

def reference(A_x, A_dft_seq_re, A_dft_seq_im, A_dft_hid_re, A_dft_hid_im, B_x, B_w_r, B_w_gate, B_w_up, B_w_down, c, w_mod):

    def _A_ref(x, dft_seq_re, dft_seq_im, dft_hid_re, dft_hid_im):
        a = torch.einsum('bsd,ed->bse', x, dft_hid_re)
        b = torch.einsum('bsd,ed->bse', x, dft_hid_im)
        y = torch.einsum('ts,bse->bte', dft_seq_re, a) - torch.einsum('ts,bse->bte', dft_seq_im, b)
        return y

    def _B_ref(x, w_r, w_gate, w_up, w_down):
        B, S, D = x.shape
        C = 2
        r = x @ w_r
        idx = torch.argsort(-r, axis=-1)[:, :C]
        idx_e = (lambda x, n, axis: torch.repeat_interleave(x, n, dim=axis))(idx.reshape(B, C, 1), D, axis=2)
        x_sel = (lambda x, i, axis: torch.take_along_dim(x, i, dim=axis))(x, idx_e, axis=1)
        g = _silu(x_sel @ w_gate)
        u = x_sel @ w_up
        y = g * u @ w_down
        r_sel = _sigmoid((lambda x, i, axis: torch.take_along_dim(x, i, dim=axis))(r, idx, axis=1))
        y = y * r_sel.reshape(B, C, 1)
        b_off = (torch.arange(B) * S).reshape(B, 1)
        flat_idx = (idx + b_off).reshape(B * C)
        zero = (x * 0.0).reshape(B * S, D)
        upd = _scatter_add(zero, flat_idx, y.reshape(B * C, D), axis=0)
        return x + upd.reshape(B, S, D)
    x = A_x
    mod = (_silu(c) @ w_mod).reshape(c.shape[0], 6, 1, -1)
    sa, ca, ga, sb, cb, gb = (mod[:, 0].clone(), mod[:, 1].clone(), mod[:, 2].clone(), mod[:, 3].clone(), mod[:, 4].clone(), mod[:, 5].clone())
    h = _layernorm(x, axis=-1) * (1 + ca) + sa
    x = x + ga * _A_ref(h, A_dft_seq_re, A_dft_seq_im, A_dft_hid_re, A_dft_hid_im)
    h = _layernorm(x, axis=-1) * (1 + cb) + sb
    x = x + gb * _B_ref(h, B_w_r, B_w_gate, B_w_up, B_w_down)
    return x
```

```cpp
#include <hip/hip_runtime.h>
#include <hip/hip_bf16.h>
#include <stdint.h>

typedef short bf16x8 __attribute__((ext_vector_type(8)));
typedef float f32x4 __attribute__((ext_vector_type(4)));
typedef unsigned u32x4 __attribute__((ext_vector_type(4)));
typedef unsigned u32x2 __attribute__((ext_vector_type(2)));
typedef unsigned short u16;

constexpr int S = 2048, D = 512, F = 1792;
constexpr float EPS = 1e-5f;

constexpr size_t OFF_NB = 0;
constexpr size_t OFF_PB = OFF_NB + (size_t)S * D * 2;
constexpr size_t OFF_X1 = OFF_PB + (size_t)S * D * 4;
constexpr size_t OFF_MODP = OFF_X1 + (size_t)S * D * 2;
constexpr size_t OFF_RSP = OFF_MODP + 4 * 3072 * 4;
constexpr size_t OFF_U = OFF_RSP + 8 * 4096 * 4;
constexpr size_t OFF_ST = OFF_U + 2 * 512 * 4;
constexpr size_t OFF_HID = OFF_ST + 3 * S * 4;
constexpr size_t OFF_SEL = OFF_HID + 2 * F * 4;

__device__ __forceinline__ float bf2f(u16 b) { return __builtin_bit_cast(float, (unsigned)b << 16); }
__device__ __forceinline__ float bflo(unsigned w) { return __builtin_bit_cast(float, w << 16); }
__device__ __forceinline__ float bfhi(unsigned w) { return __builtin_bit_cast(float, w & 0xffff0000u); }
typedef __bf16 bf16x2_t __attribute__((ext_vector_type(2)));
typedef float f32x2 __attribute__((ext_vector_type(2)));
__device__ __forceinline__ unsigned pk2(float lo, float hi) {
  f32x2 v = {lo, hi};
  bf16x2_t r = __builtin_convertvector(v, bf16x2_t);
  return __builtin_bit_cast(unsigned, r);
}
__device__ __forceinline__ float rbf(float x) { return bflo(pk2(x, 0.f)); }
__device__ __forceinline__ float wave_sum(float v) {
#pragma unroll
  for (int o = 32; o >= 1; o >>= 1) v += __shfl_xor(v, o);
  return v;
}
__device__ __forceinline__ float modv(const float* modp, int j) {
  return rbf((modp[j] + modp[3072 + j]) + (modp[2 * 3072 + j] + modp[3 * 3072 + j]));
}

#define WAIT_V(n) asm volatile("s_waitcnt vmcnt(" #n ")" ::: "memory")
#define BAR() __builtin_amdgcn_s_barrier()

__global__ __launch_bounds__(512) void k_prep(const u16* __restrict__ x, const u16* __restrict__ c,
                                              const u16* __restrict__ wmod, char* __restrict__ ws) {
  __shared__ __attribute__((aligned(16))) u16 lds[D * 8 + 64];
  const int tid = threadIdx.x, lane = tid & 63, w = tid >> 6;
  const int b = blockIdx.x;
  if (b < 256) {
    const int row = b * 8 + w;
    u32x4 v = *(const u32x4*)(x + (size_t)row * D + lane * 8);
    float f[8];
#pragma unroll
    for (int i = 0; i < 4; ++i) { f[2 * i] = bflo(v[i]); f[2 * i + 1] = bfhi(v[i]); }
    float s = 0;
#pragma unroll
    for (int i = 0; i < 8; ++i) s += f[i];
    const float m = wave_sum(s) * (1.f / D);
    float q = 0;
#pragma unroll
    for (int i = 0; i < 8; ++i) { f[i] -= m; q += f[i] * f[i]; }
    const float rs = rsqrtf(wave_sum(q) * (1.f / D) + EPS);
#pragma unroll
    for (int i = 0; i < 8; ++i) lds[(lane * 8 + i) * 8 + w] = (u16)(pk2(f[i] * rs, 0.f) & 0xffff);
    if (tid < 24) ((float*)(ws + OFF_ST))[(tid >> 3) * S + b * 8 + (tid & 7)] = 0.f;
    __syncthreads();
    u32x4 o = *(const u32x4*)(lds + tid * 8);
    *(u32x4*)(ws + OFF_NB + ((size_t)b * D + tid) * 16) = o;
  } else {
    const int j = b - 256;
    const int cs = j % 48, ks = j / 48;
    const int cg = lane & 7;
    const int rg = tid >> 3;
    float acc[8];
#pragma unroll
    for (int i = 0; i < 8; ++i) acc[i] = 0.f;
    u32x4 wv[2];
    float sc[2];
#pragma unroll
    for (int r = 0; r < 2; ++r) {
      const int k = ks * 128 + r * 64 + rg;
      wv[r] = *(const u32x4*)(wmod + (size_t)k * 3072 + cs * 64 + cg * 8);
      const float cv = bf2f(c[k]);
      sc[r] = rbf(cv / (1.f + __expf(-cv)));
    }
#pragma unroll
    for (int r = 0; r < 2; ++r)
#pragma unroll
      for (int i = 0; i < 4; ++i) { acc[2 * i] += sc[r] * bflo(wv[r][i]); acc[2 * i + 1] += sc[r] * bfhi(wv[r][i]); }
#pragma unroll
    for (int i = 0; i < 8; ++i) {
      acc[i] += __shfl_xor(acc[i], 8); acc[i] += __shfl_xor(acc[i], 16); acc[i] += __shfl_xor(acc[i], 32);
    }
    float* red = (float*)lds;
    if (lane < 8) {
#pragma unroll
      for (int i = 0; i < 8; ++i) red[w * 64 + lane * 8 + i] = acc[i];
    }
    __syncthreads();
    if (tid < 64) {
      float s = 0;
#pragma unroll
      for (int i = 0; i < 8; ++i) s += red[i * 64 + tid];
      ((float*)(ws + OFF_MODP))[ks * 3072 + cs * 64 + tid] = s;
    }
  }
}

__device__ __forceinline__ void glds16(const void* g, void* l) {
  __builtin_amdgcn_global_load_lds((const __attribute__((address_space(1))) unsigned*)g,
                                   (__attribute__((address_space(3))) unsigned*)l, 16, 0, 0);
}

constexpr int K2_ST = 6, K2_STB = 128 * 128 + 8 * 64 * 16;
__global__ __launch_bounds__(256) void k_gemm1(const u16* __restrict__ wre, const u16* __restrict__ wim,
                                               const u16* __restrict__ dre, const u16* __restrict__ dim_,
                                               char* __restrict__ ws) {
  extern __shared__ __attribute__((aligned(16))) char smem[];
  const int tid = threadIdx.x, lane = tid & 63;
  const int w = __builtin_amdgcn_readfirstlane(tid >> 6);
  const int b = blockIdx.x;
  const int xcd = b & 7, loc = b >> 3;
  const int mt = xcd * 4 + (loc >> 3);
  const int nt = loc & 7;
  const bool is_im = mt >= 16;
  const u16* W = (is_im ? wim : wre) + (size_t)(mt & 15) * 128 * S;
  const char* X = ws + OFF_NB + (size_t)nt * 64 * 16;
  const int urow = b * 4 + w;
  const u32x4 dv = *(const u32x4*)((urow < 512 ? dre : dim_) + (size_t)(urow & 511) * D + lane * 8);
  const float* modp = (const float*)(ws + OFF_MODP);
  float us = 0;
#pragma unroll
  for (int i = 0; i < 4; ++i)
    us += modv(modp, lane * 8 + 2 * i) * bflo(dv[i]) + modv(modp, lane * 8 + 2 * i + 1) * bfhi(dv[i]);
  us = wave_sum(us);
  if (lane == 0) ((float*)(ws + OFF_U))[urow] = us;
  const int wt = w >> 1, wd = w & 1;
  float c1[2][4];
#pragma unroll
  for (int df = 0; df < 2; ++df)
#pragma unroll
    for (int j = 0; j < 4; ++j) {
      float v = rbf(1.f + modv(modp, 512 + nt * 64 + wd * 32 + df * 16 + (lane >> 4) * 4 + j));
      c1[df][j] = is_im ? -v : v;
    }

  const u16* gw[4];
#pragma unroll
  for (int i = 0; i < 4; ++i) {
    const int row = (w * 4 + i) * 8 + (lane >> 3);
    const int cch = (lane & 7) ^ ((row >> 1) & 7);
    gw[i] = W + (size_t)row * S + cch * 8;
  }
  const char* gx[2];
#pragma unroll
  for (int i = 0; i < 2; ++i) gx[i] = X + (size_t)(w * 2 + i) * D * 16 + lane * 16;
  auto stage = [&](int kt, int slot) {
    char* sb = smem + slot * K2_STB;
#pragma unroll
    for (int i = 0; i < 4; ++i) glds16(gw[i] + kt * 64, sb + (w * 4 + i) * 1024);
#pragma unroll
    for (int i = 0; i < 2; ++i) glds16(gx[i] + (size_t)kt * 8 * D * 16, sb + 16384 + (w * 2 + i) * 1024);
  };
  f32x4 acc[2][4], rs[4];
#pragma unroll
  for (int i = 0; i < 2; ++i)
#pragma unroll
    for (int j = 0; j < 4; ++j) acc[i][j] = f32x4{0, 0, 0, 0};
#pragma unroll
  for (int j = 0; j < 4; ++j) rs[j] = f32x4{0, 0, 0, 0};
  const bf16x8 ones = {0x3f80, 0x3f80, 0x3f80, 0x3f80, 0x3f80, 0x3f80, 0x3f80, 0x3f80};
  const int fr = lane & 15, fq = lane >> 4;
  int offW[2];
#pragma unroll
  for (int ks = 0; ks < 2; ++ks) offW[ks] = (wt * 64 + fr) * 128 + (((ks * 4 + fq) ^ ((fr >> 1) & 7)) * 16);
  const int offX = 16384 + fq * 1024 + (wd * 32 + fr) * 16;
  constexpr int NT = S / 64;
  WAIT_V(0);
#pragma unroll
  for (int p = 0; p < K2_ST - 1; ++p) stage(p, p);
  int slot = 0;
  for (int kt = 0; kt < NT; ++kt) {
    if (kt + K2_ST - 1 <= NT) { WAIT_V(24); } else { WAIT_V(0); }
    BAR();
    if (kt + K2_ST - 1 < NT) { int ns = slot - 1; if (ns < 0) ns += K2_ST; stage(kt + K2_ST - 1, ns); }
    const char* sb = smem + slot * K2_STB;
#pragma unroll
    for (int ks = 0; ks < 2; ++ks) {
      bf16x8 wf[4], xf[2];
#pragma unroll
      for (int j = 0; j < 4; ++j) wf[j] = *(const bf16x8*)(sb + offW[ks] + j * 16 * 128);
#pragma unroll
      for (int i = 0; i < 2; ++i) xf[i] = *(const bf16x8*)(sb + offX + ks * 4096 + i * 256);
#pragma unroll
      for (int i = 0; i < 2; ++i)
#pragma unroll
        for (int j = 0; j < 4; ++j) acc[i][j] = __builtin_amdgcn_mfma_f32_16x16x32_bf16(xf[i], wf[j], acc[i][j], 0, 0, 0);
      if ((kt >> 2) == nt && wd == 0) {
#pragma unroll
        for (int j = 0; j < 4; ++j) rs[j] = __builtin_amdgcn_mfma_f32_16x16x32_bf16(ones, wf[j], rs[j], 0, 0, 0);
      }
    }
    ++slot; if (slot == K2_ST) slot = 0;
  }
  const int t0 = (mt & 15) * 128 + wt * 64;
#pragma unroll
  for (int i = 0; i < 2; ++i) {
    const int d = nt * 64 + wd * 32 + i * 16 + fq * 4;
    const int kblk = ((is_im ? 512 : 0) + d) >> 3;
#pragma unroll
    for (int j = 0; j < 4; ++j) {
      u32x2 o;
      o[0] = pk2(acc[i][j][0] * c1[i][0], acc[i][j][1] * c1[i][1]);
      o[1] = pk2(acc[i][j][2] * c1[i][2], acc[i][j][3] * c1[i][3]);
      *(u32x2*)(ws + OFF_PB + ((size_t)kblk * S + t0 + j * 16 + fr) * 16 + (fq & 1) * 8) = o;
    }
  }
  if (wd == 0 && fq == 0) {
#pragma unroll
    for (int j = 0; j < 4; ++j) ((float*)(ws + OFF_RSP))[nt * 4096 + mt * 128 + wt * 64 + j * 16 + fr] = rs[j][0];
  }
}

constexpr int K3_ST = 8, K3_STB = 64 * 128 + 8 * 64 * 16;
__global__ __launch_bounds__(256) void k_gemm2(const u16* __restrict__ dre, const u16* __restrict__ dim_,
                                               const u16* __restrict__ x, const u16* __restrict__ wr,
                                               char* __restrict__ ws) {
  extern __shared__ __attribute__((aligned(16))) char smem[];
  const int tid = threadIdx.x, lane = tid & 63;
  const int w = __builtin_amdgcn_readfirstlane(tid >> 6);
  const int b = blockIdx.x;
  const int xcd = b & 7, loc = b >> 3;
  const int tt = xcd * 4 + (loc >> 3);
  const int et = loc & 7;
  const int we = w >> 1, wt = w & 1;
  const int fr = lane & 15, fq = lane >> 4;
  const float* modp = (const float*)(ws + OFF_MODP);
  float ga[2][4], vv[2][4], ur[2][4], ui[2][4], rr[2], ri[2];
  u32x2 xv[2][2];
#pragma unroll
  for (int i = 0; i < 2; ++i)
#pragma unroll
    for (int j = 0; j < 4; ++j) {
      const int e = et * 64 + we * 32 + i * 16 + fq * 4 + j;
      ga[i][j] = modv(modp, 1024 + e);
      vv[i][j] = rbf(1.f + modv(modp, 4 * 512 + e)) * bf2f(wr[e]);
      ur[i][j] = ((const float*)(ws + OFF_U))[e];
      ui[i][j] = ((const float*)(ws + OFF_U))[512 + e];
    }
#pragma unroll
  for (int j = 0; j < 2; ++j) {
    const int t = tt * 64 + wt * 32 + j * 16 + fr;
    float a = 0, c = 0;
#pragma unroll
    for (int p = 0; p < 8; ++p) { a += ((const float*)(ws + OFF_RSP))[p * 4096 + t]; c += ((const float*)(ws + OFF_RSP))[p * 4096 + 2048 + t]; }
    rr[j] = a; ri[j] = c;
#pragma unroll
    for (int i = 0; i < 2; ++i) xv[i][j] = *(const u32x2*)(x + (size_t)t * D + et * 64 + we * 32 + i * 16 + fq * 4);
  }
  const u16* gw[2];
  size_t imoff = dim_ - dre;
#pragma unroll
  for (int i = 0; i < 2; ++i) {
    const int row = (w * 2 + i) * 8 + (lane >> 3);
    const int cch = (lane & 7) ^ ((row >> 1) & 7);
    gw[i] = dre + (size_t)(et * 64 + row) * D + cch * 8;
  }
  const char* gx[2];
#pragma unroll
  for (int i = 0; i < 2; ++i) gx[i] = ws + OFF_PB + ((size_t)(w * 2 + i) * S + tt * 64 + lane) * 16;
  auto stage = [&](int kt, int slot) {
    char* sb = smem + slot * K3_STB;
    const size_t wo = (kt >= 8 ? imoff : 0) + (kt & 7) * 64;
#pragma unroll
    for (int i = 0; i < 2; ++i) glds16(gw[i] + wo, sb + (w * 2 + i) * 1024);
#pragma unroll
    for (int i = 0; i < 2; ++i) glds16(gx[i] + (size_t)kt * 8 * S * 16, sb + 8192 + (w * 2 + i) * 1024);
  };
  f32x4 acc[2][2];
#pragma unroll
  for (int i = 0; i < 2; ++i)
#pragma unroll
    for (int j = 0; j < 2; ++j) acc[i][j] = f32x4{0, 0, 0, 0};
  int offW[2];
#pragma unroll
  for (int ks = 0; ks < 2; ++ks) offW[ks] = (we * 32 + fr) * 128 + (((ks * 4 + fq) ^ ((fr >> 1) & 7)) * 16);
  const int offX = 8192 + fq * 1024 + (wt * 32 + fr) * 16;
  constexpr int NT = 16;
  WAIT_V(0);
#pragma unroll
  for (int p = 0; p < K3_ST - 1; ++p) stage(p, p);
  int slot = 0;
  for (int kt = 0; kt < NT; ++kt) {
    if (kt + K3_ST - 1 <= NT) { WAIT_V(24); } else { WAIT_V(0); }
    BAR();
    if (kt + K3_ST - 1 < NT) { int ns = slot - 1; if (ns < 0) ns += K3_ST; stage(kt + K3_ST - 1, ns); }
    const char* sb = smem + slot * K3_STB;
#pragma unroll
    for (int ks = 0; ks < 2; ++ks) {
      bf16x8 wf[2], xf[2];
#pragma unroll
      for (int i = 0; i < 2; ++i) wf[i] = *(const bf16x8*)(sb + offW[ks] + i * 16 * 128);
#pragma unroll
      for (int j = 0; j < 2; ++j) xf[j] = *(const bf16x8*)(sb + offX + ks * 4096 + j * 256);
#pragma unroll
      for (int i = 0; i < 2; ++i)
#pragma unroll
        for (int j = 0; j < 2; ++j) acc[i][j] = __builtin_amdgcn_mfma_f32_16x16x32_bf16(wf[i], xf[j], acc[i][j], 0, 0, 0);
    }
    ++slot; if (slot == K3_ST) slot = 0;
  }
  float* st = (float*)(ws + OFF_ST);
#pragma unroll
  for (int j = 0; j < 2; ++j) {
    const int t = tt * 64 + wt * 32 + j * 16 + fr;
    float s0 = 0, s1 = 0, s2 = 0;
#pragma unroll
    for (int i = 0; i < 2; ++i) {
      float xx[4] = {bflo(xv[i][j][0]), bfhi(xv[i][j][0]), bflo(xv[i][j][1]), bfhi(xv[i][j][1])};
      float o[4];
#pragma unroll
      for (int q = 0; q < 4; ++q) {
        const float y = acc[i][j][q] + rr[j] * ur[i][q] - ri[j] * ui[i][q];
        o[q] = rbf(xx[q] + ga[i][q] * y);
        s0 += o[q]; s1 += o[q] * o[q]; s2 += o[q] * vv[i][q];
      }
      u32x2 ov; ov[0] = pk2(o[0], o[1]); ov[1] = pk2(o[2], o[3]);
      *(u32x2*)(ws + OFF_X1 + ((size_t)t * D + et * 64 + we * 32 + i * 16 + fq * 4) * 2) = ov;
    }
    s0 += __shfl_xor(s0, 16); s0 += __shfl_xor(s0, 32);
    s1 += __shfl_xor(s1, 16); s1 += __shfl_xor(s1, 32);
    s2 += __shfl_xor(s2, 16); s2 += __shfl_xor(s2, 32);
    if (fq < 3) atomicAdd(st + fq * S + t, fq == 0 ? s0 : (fq == 1 ? s1 : s2));
  }
}

struct Top2 { float v0, v1; int i0, i1; };
__device__ __forceinline__ bool better(float a, int ia, float b, int ib) { return a > b || (a == b && ia < ib); }
__device__ __forceinline__ void ins(Top2& t, float v, int i) {
  if (better(v, i, t.v0, t.i0)) { t.v1 = t.v0; t.i1 = t.i0; t.v0 = v; t.i0 = i; }
  else if (better(v, i, t.v1, t.i1)) { t.v1 = v; t.i1 = i; }
}
__global__ __launch_bounds__(256) void k_route(const u16* __restrict__ wr, const u16* __restrict__ wg,
                                               const u16* __restrict__ wu, char* __restrict__ ws, u16* __restrict__ out) {
  __shared__ float red[4 * 32 + 16];
  __shared__ int redi[8];
  const int tid = threadIdx.x, lane = tid & 63, w = tid >> 6;
  const int b = blockIdx.x;
  const float* modp = (const float*)(ws + OFF_MODP);
  const float* st = (const float*)(ws + OFF_ST);
  const u16* x1 = (const u16*)(ws + OFF_X1);
  const bool moe = (b >> 3) < 28;
  const int chunk = (b & 7) * 28 + (b >> 3);
  u32x4 gv[2], uv[2];
  if (moe) {
#pragma unroll
    for (int r = 0; r < 2; ++r) {
      gv[r] = *(const u32x4*)(wg + (size_t)(tid + r * 256) * F + chunk * 8);
      uv[r] = *(const u32x4*)(wu + (size_t)(tid + r * 256) * F + chunk * 8);
    }
  }
  float cb1[2], sb[2];
  float V1 = 0, V0 = 0;
#pragma unroll
  for (int r = 0; r < 2; ++r) {
    const int e = tid + r * 256;
    cb1[r] = rbf(1.f + modv(modp, 4 * 512 + e)); sb[r] = modv(modp, 3 * 512 + e);
    const float wv = bf2f(wr[e]);
    V1 += cb1[r] * wv; V0 += sb[r] * wv;
  }
  V1 = wave_sum(V1); V0 = wave_sum(V0);
  if (lane == 0) { red[w] = V1; red[4 + w] = V0; }
  __syncthreads();
  V1 = (red[0] + red[1]) + (red[2] + red[3]); V0 = (red[4] + red[5]) + (red[6] + red[7]);
  __syncthreads();
  Top2 t{-INFINITY, -INFINITY, 0x7fffffff, 0x7fffffff};
#pragma unroll
  for (int h = 0; h < 2; ++h) {
    const int r0 = (h * 256 + tid) * 4;
    f32x4 a = *(const f32x4*)(st + r0), q = *(const f32x4*)(st + S + r0), d = *(const f32x4*)(st + 2 * S + r0);
#pragma unroll
    for (int i = 0; i < 4; ++i) {
      const float m = a[i] * (1.f / D);
      const float var = fmaxf(q[i] * (1.f / D) - m * m, 0.f);
      const float rs = rsqrtf(var + EPS);
      ins(t, rs * (d[i] - m * V1) + V0, r0 + i);
    }
  }
#pragma unroll
  for (int o = 32; o >= 1; o >>= 1) {
    float v0 = __shfl_xor(t.v0, o), v1 = __shfl_xor(t.v1, o);
    int i0 = __shfl_xor(t.i0, o), i1 = __shfl_xor(t.i1, o);
    ins(t, v0, i0); ins(t, v1, i1);
  }
  if (lane == 0) { red[2 * w] = t.v0; red[2 * w + 1] = t.v1; redi[2 * w] = t.i0; redi[2 * w + 1] = t.i1; }
  __syncthreads();
  t = Top2{-INFINITY, -INFINITY, 0x7fffffff, 0x7fffffff};
#pragma unroll
  for (int i = 0; i < 8; ++i) ins(t, red[i], redi[i]);
  __syncthreads();
  t.i0 = min(t.i0, S - 1); t.i1 = min(t.i1, S - 1);
  if (b == 0 && tid == 0) {
    int* sel = (int*)(ws + OFF_SEL);
    sel[0] = t.i0; sel[1] = t.i1;
    ((float*)sel)[2] = t.v0; ((float*)sel)[3] = t.v1;
  }
  {
    float c8[8], s8[8], g8[8];
#pragma unroll
    for (int i = 0; i < 8; ++i) {
      const int e = lane * 8 + i;
      c8[i] = rbf(1.f + modv(modp, 4 * 512 + e)); s8[i] = modv(modp, 3 * 512 + e); g8[i] = modv(modp, 5 * 512 + e);
    }
#pragma unroll
    for (int r = 0; r < 2; ++r) {
      const int row = b * 8 + w * 2 + r;
      const float m = st[row] * (1.f / D);
      const float rs = rsqrtf(fmaxf(st[S + row] * (1.f / D) - m * m, 0.f) + EPS);
      u32x4 v = *(const u32x4*)(x1 + (size_t)row * D + lane * 8), o;
#pragma unroll
      for (int i = 0; i < 4; ++i) {
        const float a = bflo(v[i]), c = bfhi(v[i]);
        o[i] = pk2(a + g8[2 * i] * ((a - m) * rs * c8[2 * i] + s8[2 * i]),
                   c + g8[2 * i + 1] * ((c - m) * rs * c8[2 * i + 1] + s8[2 * i + 1]));
      }
      if (row != t.i0 && row != t.i1) *(u32x4*)(out + (size_t)row * D + lane * 8) = o;
    }
  }
  if (moe) {
    float acc[32];
#pragma unroll
    for (int i = 0; i < 32; ++i) acc[i] = 0.f;
#pragma unroll
    for (int k = 0; k < 2; ++k) {
      const int row = k ? t.i1 : t.i0;
      const float m = st[row] * (1.f / D);
      const float rs = rsqrtf(fmaxf(st[S + row] * (1.f / D) - m * m, 0.f) + EPS);
#pragma unroll
      for (int r = 0; r < 2; ++r) {
        const float h = rbf((bf2f(x1[(size_t)row * D + tid + r * 256]) - m) * rs * cb1[r] + sb[r]);
#pragma unroll
        for (int i = 0; i < 4; ++i) {
          acc[k * 16 + 2 * i] += h * bflo(gv[r][i]); acc[k * 16 + 2 * i + 1] += h * bfhi(gv[r][i]);
          acc[k * 16 + 8 + 2 * i] += h * bflo(uv[r][i]); acc[k * 16 + 8 + 2 * i + 1] += h * bfhi(uv[r][i]);
        }
      }
    }
#pragma unroll
    for (int i = 0; i < 32; ++i) acc[i] = wave_sum(acc[i]);
    if (lane == 0) {
#pragma unroll
      for (int i = 0; i < 32; ++i) red[w * 32 + i] = acc[i];
    }
    __syncthreads();
    if (tid < 16) {
      const int k = tid >> 3, i = tid & 7;
      float zg = 0, zu = 0;
#pragma unroll
      for (int q = 0; q < 4; ++q) { zg += red[q * 32 + k * 16 + i]; zu += red[q * 32 + k * 16 + 8 + i]; }
      zg = rbf(zg); zu = rbf(zu);
      const float g = rbf(zg / (1.f + __expf(-zg)));
      ((float*)(ws + OFF_HID))[k * F + chunk * 8 + i] = rbf(g * zu);
    }
  }
}

__global__ __launch_bounds__(256) void k_down(const u16* __restrict__ wd, char* __restrict__ ws, u16* __restrict__ out) {
  __shared__ float red[4 * 16];
  const int tid = threadIdx.x, lane = tid & 63, w = tid >> 6;
  const int b = blockIdx.x;
  const int chunk = (b & 7) * 8 + (b >> 3);
  const float* hid = (const float*)(ws + OFF_HID);
  u32x4 v[7];
#pragma unroll
  for (int r = 0; r < 7; ++r) v[r] = *(const u32x4*)(wd + (size_t)(tid + r * 256) * D + chunk * 8);
  float acc[16];
#pragma unroll
  for (int i = 0; i < 16; ++i) acc[i] = 0.f;
#pragma unroll
  for (int r = 0; r < 7; ++r) {
    const float h0 = hid[tid + r * 256], h1 = hid[F + tid + r * 256];
#pragma unroll
    for (int i = 0; i < 4; ++i) {
      acc[2 * i] += h0 * bflo(v[r][i]); acc[2 * i + 1] += h0 * bfhi(v[r][i]);
      acc[8 + 2 * i] += h1 * bflo(v[r][i]); acc[8 + 2 * i + 1] += h1 * bfhi(v[r][i]);
    }
  }
#pragma unroll
  for (int i = 0; i < 16; ++i) acc[i] = wave_sum(acc[i]);
  if (lane == 0) {
#pragma unroll
    for (int i = 0; i < 16; ++i) red[w * 16 + i] = acc[i];
  }
  __syncthreads();
  if (tid < 16) {
    const int k = tid >> 3, e = chunk * 8 + (tid & 7);
    const int* sel = (const int*)(ws + OFF_SEL);
    const int row = sel[k];
    const float rv = rbf(((const float*)sel)[2 + k]);
    const float sg = rbf(1.f / (1.f + __expf(-rv)));
    float y = rbf((red[tid] + red[16 + tid]) + (red[32 + tid] + red[48 + tid]));
    y = rbf(y * sg);
    const float* modp = (const float*)(ws + OFF_MODP);
    const float* st = (const float*)(ws + OFF_ST);
    const float m = st[row] * (1.f / D);
    const float rs = rsqrtf(fmaxf(st[S + row] * (1.f / D) - m * m, 0.f) + EPS);
    const float a = bf2f(((const u16*)(ws + OFF_X1))[(size_t)row * D + e]);
    const float h = rbf((a - m) * rs * rbf(1.f + modv(modp, 4 * 512 + e)) + modv(modp, 3 * 512 + e));
    const float o = a + rbf(modv(modp, 5 * 512 + e) * rbf(h + y));
    out[(size_t)row * D + e] = (u16)(pk2(o, 0.f) & 0xffff);
  }
}

extern "C" void kernel_launch(void* const* d_in, const int* in_sizes, int n_in, void* d_out, int out_size, void* d_ws,
                              size_t ws_size, hipStream_t stream) {
  const u16* x = (const u16*)d_in[0];
  const u16* sre = (const u16*)d_in[1];
  const u16* sim = (const u16*)d_in[2];
  const u16* dre = (const u16*)d_in[3];
  const u16* dim_ = (const u16*)d_in[4];
  const u16* wr = (const u16*)d_in[6];
  const u16* wg = (const u16*)d_in[7];
  const u16* wu = (const u16*)d_in[8];
  const u16* wd = (const u16*)d_in[9];
  const u16* c = (const u16*)d_in[10];
  const u16* wmod = (const u16*)d_in[11];
  char* ws = (char*)d_ws;
  u16* out = (u16*)d_out;
  hipFuncSetAttribute((const void*)k_gemm1, hipFuncAttributeMaxDynamicSharedMemorySize, K2_ST * K2_STB);
  hipFuncSetAttribute((const void*)k_gemm2, hipFuncAttributeMaxDynamicSharedMemorySize, K3_ST * K3_STB);
  k_prep<<<256 + 192, 512, 0, stream>>>(x, c, wmod, ws);
  k_gemm1<<<256, 256, K2_ST * K2_STB, stream>>>(sre, sim, dre, dim_, ws);
  k_gemm2<<<256, 256, K3_ST * K3_STB, stream>>>(dre, dim_, x, wr, ws);
  k_route<<<256, 256, 0, stream>>>(wr, wg, wu, ws, out);
  k_down<<<64, 256, 0, stream>>>(wd, ws, out);
}
```

```cpp
#define STAMPS
#define LEAK_K 0
#define LEAK_J 0
#define LEAK_G 448
#define LEAK_MAX 0
#include <hip/hip_runtime.h>
#include <hip/hip_bf16.h>
#include <stdint.h>

typedef short bf16x8 __attribute__((ext_vector_type(8)));
typedef float f32x4 __attribute__((ext_vector_type(4)));
typedef unsigned u32x4 __attribute__((ext_vector_type(4)));
typedef unsigned u32x2 __attribute__((ext_vector_type(2)));
typedef unsigned short u16;

constexpr int S = 2048, D = 512, F = 1792;
constexpr float EPS = 1e-5f;

constexpr size_t OFF_NB = 0;
constexpr size_t OFF_PB = OFF_NB + (size_t)S * D * 2;
constexpr size_t OFF_X1 = OFF_PB + (size_t)S * D * 4;
constexpr size_t OFF_MODP = OFF_X1 + (size_t)S * D * 2;
constexpr size_t OFF_RSP = OFF_MODP + 4 * 3072 * 4;
constexpr size_t OFF_U = OFF_RSP + 8 * 4096 * 4;
constexpr size_t OFF_ST = OFF_U + 2 * 512 * 4;
constexpr size_t OFF_HID = OFF_ST + 3 * S * 4;
constexpr size_t OFF_SEL = OFF_HID + 2 * F * 4;

__device__ __forceinline__ float bf2f(u16 b) { return __builtin_bit_cast(float, (unsigned)b << 16); }
__device__ __forceinline__ float bflo(unsigned w) { return __builtin_bit_cast(float, w << 16); }
__device__ __forceinline__ float bfhi(unsigned w) { return __builtin_bit_cast(float, w & 0xffff0000u); }
typedef __bf16 bf16x2_t __attribute__((ext_vector_type(2)));
typedef float f32x2 __attribute__((ext_vector_type(2)));
__device__ __forceinline__ unsigned pk2(float lo, float hi) {
  f32x2 v = {lo, hi};
  bf16x2_t r = __builtin_convertvector(v, bf16x2_t);
  return __builtin_bit_cast(unsigned, r);
}
__device__ __forceinline__ float rbf(float x) { return bflo(pk2(x, 0.f)); }
__device__ __forceinline__ float wave_sum(float v) {
#pragma unroll
  for (int o = 32; o >= 1; o >>= 1) v += __shfl_xor(v, o);
  return v;
}
__device__ __forceinline__ float modv(const float* modp, int j) {
  return rbf((modp[j] + modp[3072 + j]) + (modp[2 * 3072 + j] + modp[3 * 3072 + j]));
}

#define NSTK 8
constexpr size_t STAMP_OFF = 128u << 20;
static const int stamp_grid[8] = {448, 256, 256, 256, 64, 0, 0, 0};
static const int stamp_n[8] = {2, 4, 4, 4, 3, 0, 0, 0};
#define STAMP(k, j) do { if (threadIdx.x == 0) ((unsigned long long*)(ws + STAMP_OFF))[((size_t)(k) * 1024 + blockIdx.x) * NSTK + (j)] = __builtin_amdgcn_s_memrealtime(); } while (0)
#define WAIT_V(n) asm volatile("s_waitcnt vmcnt(" #n ")" ::: "memory")
#define BAR() __builtin_amdgcn_s_barrier()

__global__ __launch_bounds__(512) void k_prep(const u16* __restrict__ x, const u16* __restrict__ c,
                                              const u16* __restrict__ wmod, char* __restrict__ ws) {
  __shared__ __attribute__((aligned(16))) u16 lds[D * 8 + 64];
  const int tid = threadIdx.x, lane = tid & 63, w = tid >> 6;
  const int b = blockIdx.x;
  STAMP(0, 0);
  if (b < 256) {
    const int row = b * 8 + w;
    u32x4 v = *(const u32x4*)(x + (size_t)row * D + lane * 8);
    float f[8];
#pragma unroll
    for (int i = 0; i < 4; ++i) { f[2 * i] = bflo(v[i]); f[2 * i + 1] = bfhi(v[i]); }
    float s = 0;
#pragma unroll
    for (int i = 0; i < 8; ++i) s += f[i];
    const float m = wave_sum(s) * (1.f / D);
    float q = 0;
#pragma unroll
    for (int i = 0; i < 8; ++i) { f[i] -= m; q += f[i] * f[i]; }
    const float rs = rsqrtf(wave_sum(q) * (1.f / D) + EPS);
#pragma unroll
    for (int i = 0; i < 8; ++i) lds[(lane * 8 + i) * 8 + w] = (u16)(pk2(f[i] * rs, 0.f) & 0xffff);
    if (tid < 24) ((float*)(ws + OFF_ST))[(tid >> 3) * S + b * 8 + (tid & 7)] = 0.f;
    __syncthreads();
    u32x4 o = *(const u32x4*)(lds + tid * 8);
    *(u32x4*)(ws + OFF_NB + ((size_t)b * D + tid) * 16) = o;
  } else {
    const int j = b - 256;
    const int cs = j % 48, ks = j / 48;
    const int cg = lane & 7;
    const int rg = tid >> 3;
    float acc[8];
#pragma unroll
    for (int i = 0; i < 8; ++i) acc[i] = 0.f;
    u32x4 wv[2];
    float sc[2];
#pragma unroll
    for (int r = 0; r < 2; ++r) {
      const int k = ks * 128 + r * 64 + rg;
      wv[r] = *(const u32x4*)(wmod + (size_t)k * 3072 + cs * 64 + cg * 8);
      const float cv = bf2f(c[k]);
      sc[r] = rbf(cv / (1.f + __expf(-cv)));
    }
#pragma unroll
    for (int r = 0; r < 2; ++r)
#pragma unroll
      for (int i = 0; i < 4; ++i) { acc[2 * i] += sc[r] * bflo(wv[r][i]); acc[2 * i + 1] += sc[r] * bfhi(wv[r][i]); }
#pragma unroll
    for (int i = 0; i < 8; ++i) {
      acc[i] += __shfl_xor(acc[i], 8); acc[i] += __shfl_xor(acc[i], 16); acc[i] += __shfl_xor(acc[i], 32);
    }
    float* red = (float*)lds;
    if (lane < 8) {
#pragma unroll
      for (int i = 0; i < 8; ++i) red[w * 64 + lane * 8 + i] = acc[i];
    }
    __syncthreads();
    if (tid < 64) {
      float s = 0;
#pragma unroll
      for (int i = 0; i < 8; ++i) s += red[i * 64 + tid];
      ((float*)(ws + OFF_MODP))[ks * 3072 + cs * 64 + tid] = s;
    }
  }
  STAMP(0, 1);
}

__device__ __forceinline__ void glds16(const void* g, void* l) {
  __builtin_amdgcn_global_load_lds((const __attribute__((address_space(1))) unsigned*)g,
                                   (__attribute__((address_space(3))) unsigned*)l, 16, 0, 0);
}

constexpr int K2_ST = 6, K2_STB = 128 * 128 + 8 * 64 * 16;
__global__ __launch_bounds__(256) void k_gemm1(const u16* __restrict__ wre, const u16* __restrict__ wim,
                                               const u16* __restrict__ dre, const u16* __restrict__ dim_,
                                               char* __restrict__ ws) {
  extern __shared__ __attribute__((aligned(16))) char smem[];
  const int tid = threadIdx.x, lane = tid & 63;
  const int w = __builtin_amdgcn_readfirstlane(tid >> 6);
  const int b = blockIdx.x;
  STAMP(1, 0);
  const int xcd = b & 7, loc = b >> 3;
  const int mt = xcd * 4 + (loc >> 3);
  const int nt = loc & 7;
  const bool is_im = mt >= 16;
  const u16* W = (is_im ? wim : wre) + (size_t)(mt & 15) * 128 * S;
  const char* X = ws + OFF_NB + (size_t)nt * 64 * 16;
  const int urow = b * 4 + w;
  const u32x4 dv = *(const u32x4*)((urow < 512 ? dre : dim_) + (size_t)(urow & 511) * D + lane * 8);
  const float* modp = (const float*)(ws + OFF_MODP);
  float us = 0;
#pragma unroll
  for (int i = 0; i < 4; ++i)
    us += modv(modp, lane * 8 + 2 * i) * bflo(dv[i]) + modv(modp, lane * 8 + 2 * i + 1) * bfhi(dv[i]);
  us = wave_sum(us);
  if (lane == 0) ((float*)(ws + OFF_U))[urow] = us;
  const int wt = w >> 1, wd = w & 1;
  float c1[2][4];
#pragma unroll
  for (int df = 0; df < 2; ++df)
#pragma unroll
    for (int j = 0; j < 4; ++j) {
      float v = rbf(1.f + modv(modp, 512 + nt * 64 + wd * 32 + df * 16 + (lane >> 4) * 4 + j));
      c1[df][j] = is_im ? -v : v;
    }

  const u16* gw[4];
#pragma unroll
  for (int i = 0; i < 4; ++i) {
    const int row = (w * 4 + i) * 8 + (lane >> 3);
    const int cch = (lane & 7) ^ ((row >> 1) & 7);
    gw[i] = W + (size_t)row * S + cch * 8;
  }
  const char* gx[2];
#pragma unroll
  for (int i = 0; i < 2; ++i) gx[i] = X + (size_t)(w * 2 + i) * D * 16 + lane * 16;
  auto stage = [&](int kt, int slot) {
    char* sb = smem + slot * K2_STB;
#pragma unroll
    for (int i = 0; i < 4; ++i) glds16(gw[i] + kt * 64, sb + (w * 4 + i) * 1024);
#pragma unroll
    for (int i = 0; i < 2; ++i) glds16(gx[i] + (size_t)kt * 8 * D * 16, sb + 16384 + (w * 2 + i) * 1024);
  };
  f32x4 acc[2][4], rs[4];
#pragma unroll
  for (int i = 0; i < 2; ++i)
#pragma unroll
    for (int j = 0; j < 4; ++j) acc[i][j] = f32x4{0, 0, 0, 0};
#pragma unroll
  for (int j = 0; j < 4; ++j) rs[j] = f32x4{0, 0, 0, 0};
  const bf16x8 ones = {0x3f80, 0x3f80, 0x3f80, 0x3f80, 0x3f80, 0x3f80, 0x3f80, 0x3f80};
  const int fr = lane & 15, fq = lane >> 4;
  int offW[2];
#pragma unroll
  for (int ks = 0; ks < 2; ++ks) offW[ks] = (wt * 64 + fr) * 128 + (((ks * 4 + fq) ^ ((fr >> 1) & 7)) * 16);
  const int offX = 16384 + fq * 1024 + (wd * 32 + fr) * 16;
  constexpr int NT = S / 64;
  STAMP(1, 1);
  WAIT_V(0);
#pragma unroll
  for (int p = 0; p < K2_ST - 1; ++p) stage(p, p);
  int slot = 0;
  for (int kt = 0; kt < NT; ++kt) {
    if (kt + K2_ST - 1 <= NT) { WAIT_V(24); } else { WAIT_V(0); }
    BAR();
    if (kt + K2_ST - 1 < NT) { int ns = slot - 1; if (ns < 0) ns += K2_ST; stage(kt + K2_ST - 1, ns); }
    const char* sb = smem + slot * K2_STB;
#pragma unroll
    for (int ks = 0; ks < 2; ++ks) {
      bf16x8 wf[4], xf[2];
#pragma unroll
      for (int j = 0; j < 4; ++j) wf[j] = *(const bf16x8*)(sb + offW[ks] + j * 16 * 128);
#pragma unroll
      for (int i = 0; i < 2; ++i) xf[i] = *(const bf16x8*)(sb + offX + ks * 4096 + i * 256);
#pragma unroll
      for (int i = 0; i < 2; ++i)
#pragma unroll
        for (int j = 0; j < 4; ++j) acc[i][j] = __builtin_amdgcn_mfma_f32_16x16x32_bf16(xf[i], wf[j], acc[i][j], 0, 0, 0);
      if ((kt >> 2) == nt && wd == 0) {
#pragma unroll
        for (int j = 0; j < 4; ++j) rs[j] = __builtin_amdgcn_mfma_f32_16x16x32_bf16(ones, wf[j], rs[j], 0, 0, 0);
      }
    }
    ++slot; if (slot == K2_ST) slot = 0;
  }
  STAMP(1, 2);
  const int t0 = (mt & 15) * 128 + wt * 64;
#pragma unroll
  for (int i = 0; i < 2; ++i) {
    const int d = nt * 64 + wd * 32 + i * 16 + fq * 4;
    const int kblk = ((is_im ? 512 : 0) + d) >> 3;
#pragma unroll
    for (int j = 0; j < 4; ++j) {
      u32x2 o;
      o[0] = pk2(acc[i][j][0] * c1[i][0], acc[i][j][1] * c1[i][1]);
      o[1] = pk2(acc[i][j][2] * c1[i][2], acc[i][j][3] * c1[i][3]);
      *(u32x2*)(ws + OFF_PB + ((size_t)kblk * S + t0 + j * 16 + fr) * 16 + (fq & 1) * 8) = o;
    }
  }
  if (wd == 0 && fq == 0) {
#pragma unroll
    for (int j = 0; j < 4; ++j) ((float*)(ws + OFF_RSP))[nt * 4096 + mt * 128 + wt * 64 + j * 16 + fr] = rs[j][0];
  }
  STAMP(1, 3);
}

constexpr int K3_ST = 8, K3_STB = 64 * 128 + 8 * 64 * 16;
__global__ __launch_bounds__(256) void k_gemm2(const u16* __restrict__ dre, const u16* __restrict__ dim_,
                                               const u16* __restrict__ x, const u16* __restrict__ wr,
                                               char* __restrict__ ws) {
  extern __shared__ __attribute__((aligned(16))) char smem[];
  const int tid = threadIdx.x, lane = tid & 63;
  const int w = __builtin_amdgcn_readfirstlane(tid >> 6);
  const int b = blockIdx.x;
  STAMP(2, 0);
  const int xcd = b & 7, loc = b >> 3;
  const int tt = xcd * 4 + (loc >> 3);
  const int et = loc & 7;
  const int we = w >> 1, wt = w & 1;
  const int fr = lane & 15, fq = lane >> 4;
  const float* modp = (const float*)(ws + OFF_MODP);
  float ga[2][4], vv[2][4], ur[2][4], ui[2][4], rr[2], ri[2];
  u32x2 xv[2][2];
#pragma unroll
  for (int i = 0; i < 2; ++i)
#pragma unroll
    for (int j = 0; j < 4; ++j) {
      const int e = et * 64 + we * 32 + i * 16 + fq * 4 + j;
      ga[i][j] = modv(modp, 1024 + e);
      vv[i][j] = rbf(1.f + modv(modp, 4 * 512 + e)) * bf2f(wr[e]);
      ur[i][j] = ((const float*)(ws + OFF_U))[e];
      ui[i][j] = ((const float*)(ws + OFF_U))[512 + e];
    }
#pragma unroll
  for (int j = 0; j < 2; ++j) {
    const int t = tt * 64 + wt * 32 + j * 16 + fr;
    float a = 0, c = 0;
#pragma unroll
    for (int p = 0; p < 8; ++p) { a += ((const float*)(ws + OFF_RSP))[p * 4096 + t]; c += ((const float*)(ws + OFF_RSP))[p * 4096 + 2048 + t]; }
    rr[j] = a; ri[j] = c;
#pragma unroll
    for (int i = 0; i < 2; ++i) xv[i][j] = *(const u32x2*)(x + (size_t)t * D + et * 64 + we * 32 + i * 16 + fq * 4);
  }
  const u16* gw[2];
  size_t imoff = dim_ - dre;
#pragma unroll
  for (int i = 0; i < 2; ++i) {
    const int row = (w * 2 + i) * 8 + (lane >> 3);
    const int cch = (lane & 7) ^ ((row >> 1) & 7);
    gw[i] = dre + (size_t)(et * 64 + row) * D + cch * 8;
  }
  const char* gx[2];
#pragma unroll
  for (int i = 0; i < 2; ++i) gx[i] = ws + OFF_PB + ((size_t)(w * 2 + i) * S + tt * 64 + lane) * 16;
  auto stage = [&](int kt, int slot) {
    char* sb = smem + slot * K3_STB;
    const size_t wo = (kt >= 8 ? imoff : 0) + (kt & 7) * 64;
#pragma unroll
    for (int i = 0; i < 2; ++i) glds16(gw[i] + wo, sb + (w * 2 + i) * 1024);
#pragma unroll
    for (int i = 0; i < 2; ++i) glds16(gx[i] + (size_t)kt * 8 * S * 16, sb + 8192 + (w * 2 + i) * 1024);
  };
  f32x4 acc[2][2];
#pragma unroll
  for (int i = 0; i < 2; ++i)
#pragma unroll
    for (int j = 0; j < 2; ++j) acc[i][j] = f32x4{0, 0, 0, 0};
  int offW[2];
#pragma unroll
  for (int ks = 0; ks < 2; ++ks) offW[ks] = (we * 32 + fr) * 128 + (((ks * 4 + fq) ^ ((fr >> 1) & 7)) * 16);
  const int offX = 8192 + fq * 1024 + (wt * 32 + fr) * 16;
  constexpr int NT = 16;
  STAMP(2, 1);
  WAIT_V(0);
#pragma unroll
  for (int p = 0; p < K3_ST - 1; ++p) stage(p, p);
  int slot = 0;
  for (int kt = 0; kt < NT; ++kt) {
    if (kt + K3_ST - 1 <= NT) { WAIT_V(24); } else { WAIT_V(0); }
    BAR();
    if (kt + K3_ST - 1 < NT) { int ns = slot - 1; if (ns < 0) ns += K3_ST; stage(kt + K3_ST - 1, ns); }
    const char* sb = smem + slot * K3_STB;
#pragma unroll
    for (int ks = 0; ks < 2; ++ks) {
      bf16x8 wf[2], xf[2];
#pragma unroll
      for (int i = 0; i < 2; ++i) wf[i] = *(const bf16x8*)(sb + offW[ks] + i * 16 * 128);
#pragma unroll
      for (int j = 0; j < 2; ++j) xf[j] = *(const bf16x8*)(sb + offX + ks * 4096 + j * 256);
#pragma unroll
      for (int i = 0; i < 2; ++i)
#pragma unroll
        for (int j = 0; j < 2; ++j) acc[i][j] = __builtin_amdgcn_mfma_f32_16x16x32_bf16(wf[i], xf[j], acc[i][j], 0, 0, 0);
    }
    ++slot; if (slot == K3_ST) slot = 0;
  }
  STAMP(2, 2);
  float* st = (float*)(ws + OFF_ST);
#pragma unroll
  for (int j = 0; j < 2; ++j) {
    const int t = tt * 64 + wt * 32 + j * 16 + fr;
    float s0 = 0, s1 = 0, s2 = 0;
#pragma unroll
    for (int i = 0; i < 2; ++i) {
      float xx[4] = {bflo(xv[i][j][0]), bfhi(xv[i][j][0]), bflo(xv[i][j][1]), bfhi(xv[i][j][1])};
      float o[4];
#pragma unroll
      for (int q = 0; q < 4; ++q) {
        const float y = acc[i][j][q] + rr[j] * ur[i][q] - ri[j] * ui[i][q];
        o[q] = rbf(xx[q] + ga[i][q] * y);
        s0 += o[q]; s1 += o[q] * o[q]; s2 += o[q] * vv[i][q];
      }
      u32x2 ov; ov[0] = pk2(o[0], o[1]); ov[1] = pk2(o[2], o[3]);
      *(u32x2*)(ws + OFF_X1 + ((size_t)t * D + et * 64 + we * 32 + i * 16 + fq * 4) * 2) = ov;
    }
    s0 += __shfl_xor(s0, 16); s0 += __shfl_xor(s0, 32);
    s1 += __shfl_xor(s1, 16); s1 += __shfl_xor(s1, 32);
    s2 += __shfl_xor(s2, 16); s2 += __shfl_xor(s2, 32);
    if (fq < 3) atomicAdd(st + fq * S + t, fq == 0 ? s0 : (fq == 1 ? s1 : s2));
  }
  STAMP(2, 3);
}

struct Top2 { float v0, v1; int i0, i1; };
__device__ __forceinline__ bool better(float a, int ia, float b, int ib) { return a > b || (a == b && ia < ib); }
__device__ __forceinline__ void ins(Top2& t, float v, int i) {
  if (better(v, i, t.v0, t.i0)) { t.v1 = t.v0; t.i1 = t.i0; t.v0 = v; t.i0 = i; }
  else if (better(v, i, t.v1, t.i1)) { t.v1 = v; t.i1 = i; }
}
__global__ __launch_bounds__(256) void k_route(const u16* __restrict__ wr, const u16* __restrict__ wg,
                                               const u16* __restrict__ wu, char* __restrict__ ws, u16* __restrict__ out) {
  __shared__ float red[4 * 32 + 16];
  __shared__ int redi[8];
  const int tid = threadIdx.x, lane = tid & 63, w = tid >> 6;
  const int b = blockIdx.x;
  const float* modp = (const float*)(ws + OFF_MODP);
  const float* st = (const float*)(ws + OFF_ST);
  const u16* x1 = (const u16*)(ws + OFF_X1);
  STAMP(3, 0);
  const bool moe = (b >> 3) < 28;
  const int chunk = (b & 7) * 28 + (b >> 3);
  u32x4 gv[2], uv[2];
  if (moe) {
#pragma unroll
    for (int r = 0; r < 2; ++r) {
      gv[r] = *(const u32x4*)(wg + (size_t)(tid + r * 256) * F + chunk * 8);
      uv[r] = *(const u32x4*)(wu + (size_t)(tid + r * 256) * F + chunk * 8);
    }
  }
  float cb1[2], sb[2];
  float V1 = 0, V0 = 0;
#pragma unroll
  for (int r = 0; r < 2; ++r) {
    const int e = tid + r * 256;
    cb1[r] = rbf(1.f + modv(modp, 4 * 512 + e)); sb[r] = modv(modp, 3 * 512 + e);
    const float wv = bf2f(wr[e]);
    V1 += cb1[r] * wv; V0 += sb[r] * wv;
  }
  V1 = wave_sum(V1); V0 = wave_sum(V0);
  if (lane == 0) { red[w] = V1; red[4 + w] = V0; }
  __syncthreads();
  V1 = (red[0] + red[1]) + (red[2] + red[3]); V0 = (red[4] + red[5]) + (red[6] + red[7]);
  __syncthreads();
  Top2 t{-INFINITY, -INFINITY, 0x7fffffff, 0x7fffffff};
#pragma unroll
  for (int h = 0; h < 2; ++h) {
    const int r0 = (h * 256 + tid) * 4;
    f32x4 a = *(const f32x4*)(st + r0), q = *(const f32x4*)(st + S + r0), d = *(const f32x4*)(st + 2 * S + r0);
#pragma unroll
    for (int i = 0; i < 4; ++i) {
      const float m = a[i] * (1.f / D);
      const float var = fmaxf(q[i] * (1.f / D) - m * m, 0.f);
      const float rs = rsqrtf(var + EPS);
      ins(t, rs * (d[i] - m * V1) + V0, r0 + i);
    }
  }
#pragma unroll
  for (int o = 32; o >= 1; o >>= 1) {
    float v0 = __shfl_xor(t.v0, o), v1 = __shfl_xor(t.v1, o);
    int i0 = __shfl_xor(t.i0, o), i1 = __shfl_xor(t.i1, o);
    ins(t, v0, i0); ins(t, v1, i1);
  }
  if (lane == 0) { red[2 * w] = t.v0; red[2 * w + 1] = t.v1; redi[2 * w] = t.i0; redi[2 * w + 1] = t.i1; }
  __syncthreads();
  t = Top2{-INFINITY, -INFINITY, 0x7fffffff, 0x7fffffff};
#pragma unroll
  for (int i = 0; i < 8; ++i) ins(t, red[i], redi[i]);
  __syncthreads();
  STAMP(3, 1);
  t.i0 = min(t.i0, S - 1); t.i1 = min(t.i1, S - 1);
  if (b == 0 && tid == 0) {
    int* sel = (int*)(ws + OFF_SEL);
    sel[0] = t.i0; sel[1] = t.i1;
    ((float*)sel)[2] = t.v0; ((float*)sel)[3] = t.v1;
  }
  {
    float c8[8], s8[8], g8[8];
#pragma unroll
    for (int i = 0; i < 8; ++i) {
      const int e = lane * 8 + i;
      c8[i] = rbf(1.f + modv(modp, 4 * 512 + e)); s8[i] = modv(modp, 3 * 512 + e); g8[i] = modv(modp, 5 * 512 + e);
    }
#pragma unroll
    for (int r = 0; r < 2; ++r) {
      const int row = b * 8 + w * 2 + r;
      const float m = st[row] * (1.f / D);
      const float rs = rsqrtf(fmaxf(st[S + row] * (1.f / D) - m * m, 0.f) + EPS);
      u32x4 v = *(const u32x4*)(x1 + (size_t)row * D + lane * 8), o;
#pragma unroll
      for (int i = 0; i < 4; ++i) {
        const float a = bflo(v[i]), c = bfhi(v[i]);
        o[i] = pk2(a + g8[2 * i] * ((a - m) * rs * c8[2 * i] + s8[2 * i]),
                   c + g8[2 * i + 1] * ((c - m) * rs * c8[2 * i + 1] + s8[2 * i + 1]));
      }
      if (row != t.i0 && row != t.i1) *(u32x4*)(out + (size_t)row * D + lane * 8) = o;
    }
  }
  STAMP(3, 2);
  if (moe) {
    float acc[32];
#pragma unroll
    for (int i = 0; i < 32; ++i) acc[i] = 0.f;
#pragma unroll
    for (int k = 0; k < 2; ++k) {
      const int row = k ? t.i1 : t.i0;
      const float m = st[row] * (1.f / D);
      const float rs = rsqrtf(fmaxf(st[S + row] * (1.f / D) - m * m, 0.f) + EPS);
#pragma unroll
      for (int r = 0; r < 2; ++r) {
        const float h = rbf((bf2f(x1[(size_t)row * D + tid + r * 256]) - m) * rs * cb1[r] + sb[r]);
#pragma unroll
        for (int i = 0; i < 4; ++i) {
          acc[k * 16 + 2 * i] += h * bflo(gv[r][i]); acc[k * 16 + 2 * i + 1] += h * bfhi(gv[r][i]);
          acc[k * 16 + 8 + 2 * i] += h * bflo(uv[r][i]); acc[k * 16 + 8 + 2 * i + 1] += h * bfhi(uv[r][i]);
        }
      }
    }
#pragma unroll
    for (int i = 0; i < 32; ++i) acc[i] = wave_sum(acc[i]);
    if (lane == 0) {
#pragma unroll
      for (int i = 0; i < 32; ++i) red[w * 32 + i] = acc[i];
    }
    __syncthreads();
    if (tid < 16) {
      const int k = tid >> 3, i = tid & 7;
      float zg = 0, zu = 0;
#pragma unroll
      for (int q = 0; q < 4; ++q) { zg += red[q * 32 + k * 16 + i]; zu += red[q * 32 + k * 16 + 8 + i]; }
      zg = rbf(zg); zu = rbf(zu);
      const float g = rbf(zg / (1.f + __expf(-zg)));
      ((float*)(ws + OFF_HID))[k * F + chunk * 8 + i] = rbf(g * zu);
    }
  }
  STAMP(3, 3);
}

__global__ __launch_bounds__(256) void k_down(const u16* __restrict__ wd, char* __restrict__ ws, u16* __restrict__ out) {
  __shared__ float red[4 * 16];
  const int tid = threadIdx.x, lane = tid & 63, w = tid >> 6;
  const int b = blockIdx.x;
  STAMP(4, 0);
  const int chunk = (b & 7) * 8 + (b >> 3);
  const float* hid = (const float*)(ws + OFF_HID);
  u32x4 v[7];
#pragma unroll
  for (int r = 0; r < 7; ++r) v[r] = *(const u32x4*)(wd + (size_t)(tid + r * 256) * D + chunk * 8);
  float acc[16];
#pragma unroll
  for (int i = 0; i < 16; ++i) acc[i] = 0.f;
#pragma unroll
  for (int r = 0; r < 7; ++r) {
    const float h0 = hid[tid + r * 256], h1 = hid[F + tid + r * 256];
#pragma unroll
    for (int i = 0; i < 4; ++i) {
      acc[2 * i] += h0 * bflo(v[r][i]); acc[2 * i + 1] += h0 * bfhi(v[r][i]);
      acc[8 + 2 * i] += h1 * bflo(v[r][i]); acc[8 + 2 * i + 1] += h1 * bfhi(v[r][i]);
    }
  }
#pragma unroll
  for (int i = 0; i < 16; ++i) acc[i] = wave_sum(acc[i]);
  if (lane == 0) {
#pragma unroll
    for (int i = 0; i < 16; ++i) red[w * 16 + i] = acc[i];
  }
  __syncthreads();
  STAMP(4, 1);
  if (tid < 16) {
    const int k = tid >> 3, e = chunk * 8 + (tid & 7);
    const int* sel = (const int*)(ws + OFF_SEL);
    const int row = sel[k];
    const float rv = rbf(((const float*)sel)[2 + k]);
    const float sg = rbf(1.f / (1.f + __expf(-rv)));
    float y = rbf((red[tid] + red[16 + tid]) + (red[32 + tid] + red[48 + tid]));
    y = rbf(y * sg);
    const float* modp = (const float*)(ws + OFF_MODP);
    const float* st = (const float*)(ws + OFF_ST);
    const float m = st[row] * (1.f / D);
    const float rs = rsqrtf(fmaxf(st[S + row] * (1.f / D) - m * m, 0.f) + EPS);
    const float a = bf2f(((const u16*)(ws + OFF_X1))[(size_t)row * D + e]);
    const float h = rbf((a - m) * rs * rbf(1.f + modv(modp, 4 * 512 + e)) + modv(modp, 3 * 512 + e));
    const float o = a + rbf(modv(modp, 5 * 512 + e) * rbf(h + y));
    out[(size_t)row * D + e] = (u16)(pk2(o, 0.f) & 0xffff);
  }
  STAMP(4, 2);
}

__global__ void k_leak(char* ws) {
  const unsigned long long* sb = (const unsigned long long*)(ws + STAMP_OFF);
  unsigned long long t0 = ~0ull, t1 = LEAK_MAX ? 0ull : ~0ull;
  for (int b = threadIdx.x; b < 448; b += 64) t0 = min(t0, sb[(size_t)b * NSTK]);
  for (int b = threadIdx.x; b < LEAK_G; b += 64) { unsigned long long v = sb[((size_t)LEAK_K * 1024 + b) * NSTK + LEAK_J]; t1 = LEAK_MAX ? max(t1, v) : min(t1, v); }
  for (int o = 32; o >= 1; o >>= 1) {
    unsigned long long a = __shfl_xor(t0, o), c = __shfl_xor(t1, o);
    t0 = min(t0, a); t1 = LEAK_MAX ? max(t1, c) : min(t1, c);
  }
  unsigned long long d = (t1 - t0) * 16; if (d > 200000) d = 200000;
  unsigned long long s0 = __builtin_amdgcn_s_memrealtime();
  while (__builtin_amdgcn_s_memrealtime() - s0 < d) __builtin_amdgcn_s_sleep(1);
}
extern "C" void kernel_launch(void* const* d_in, const int* in_sizes, int n_in, void* d_out, int out_size, void* d_ws,
                              size_t ws_size, hipStream_t stream) {
  const u16* x = (const u16*)d_in[0];
  const u16* sre = (const u16*)d_in[1];
  const u16* sim = (const u16*)d_in[2];
  const u16* dre = (const u16*)d_in[3];
  const u16* dim_ = (const u16*)d_in[4];
  const u16* wr = (const u16*)d_in[6];
  const u16* wg = (const u16*)d_in[7];
  const u16* wu = (const u16*)d_in[8];
  const u16* wd = (const u16*)d_in[9];
  const u16* c = (const u16*)d_in[10];
  const u16* wmod = (const u16*)d_in[11];
  char* ws = (char*)d_ws;
  u16* out = (u16*)d_out;
  hipFuncSetAttribute((const void*)k_gemm1, hipFuncAttributeMaxDynamicSharedMemorySize, K2_ST * K2_STB);
  hipFuncSetAttribute((const void*)k_gemm2, hipFuncAttributeMaxDynamicSharedMemorySize, K3_ST * K3_STB);
  k_prep<<<256 + 192, 512, 0, stream>>>(x, c, wmod, ws);
  k_gemm1<<<256, 256, K2_ST * K2_STB, stream>>>(sre, sim, dre, dim_, ws);
  k_gemm2<<<256, 256, K3_ST * K3_STB, stream>>>(dre, dim_, x, wr, ws);
  k_route<<<256, 256, 0, stream>>>(wr, wg, wu, ws, out);
  k_down<<<64, 256, 0, stream>>>(wd, ws, out);
  k_leak<<<1, 64, 0, stream>>>(ws);
}
```

```cpp
#define PROT 0
#define PAUX 0
#define LEAK_ON 0
#include <hip/hip_runtime.h>
#include <hip/hip_bf16.h>
#include <stdint.h>

typedef short bf16x8 __attribute__((ext_vector_type(8)));
typedef float f32x4 __attribute__((ext_vector_type(4)));
typedef unsigned u32x4 __attribute__((ext_vector_type(4)));
typedef unsigned u32x2 __attribute__((ext_vector_type(2)));
typedef unsigned short u16;

constexpr int S = 2048, D = 512, F = 1792;
constexpr float EPS = 1e-5f;

constexpr size_t OFF_NB = 0;
constexpr size_t OFF_PB = OFF_NB + (size_t)S * D * 2;
constexpr size_t OFF_X1 = OFF_PB + (size_t)S * D * 4;
constexpr size_t OFF_MODP = OFF_X1 + (size_t)S * D * 2;
constexpr size_t OFF_RSP = OFF_MODP + 4 * 3072 * 4;
constexpr size_t OFF_U = OFF_RSP + 8 * 4096 * 4;
constexpr size_t OFF_ST = OFF_U + 2 * 512 * 4;
constexpr size_t OFF_HID = OFF_ST + 3 * S * 4;
constexpr size_t OFF_SEL = OFF_HID + 2 * F * 4;

__device__ __forceinline__ float bf2f(u16 b) { return __builtin_bit_cast(float, (unsigned)b << 16); }
__device__ __forceinline__ float bflo(unsigned w) { return __builtin_bit_cast(float, w << 16); }
__device__ __forceinline__ float bfhi(unsigned w) { return __builtin_bit_cast(float, w & 0xffff0000u); }
typedef __bf16 bf16x2_t __attribute__((ext_vector_type(2)));
typedef float f32x2 __attribute__((ext_vector_type(2)));
__device__ __forceinline__ unsigned pk2(float lo, float hi) {
  f32x2 v = {lo, hi};
  bf16x2_t r = __builtin_convertvector(v, bf16x2_t);
  return __builtin_bit_cast(unsigned, r);
}
__device__ __forceinline__ float rbf(float x) { return bflo(pk2(x, 0.f)); }
__device__ __forceinline__ float wave_sum(float v) {
#pragma unroll
  for (int o = 32; o >= 1; o >>= 1) v += __shfl_xor(v, o);
  return v;
}
__device__ __forceinline__ float modv(const float* modp, int j) {
  return rbf((modp[j] + modp[3072 + j]) + (modp[2 * 3072 + j] + modp[3 * 3072 + j]));
}

#define STAMP(k, j)
#define WAIT_V(n) asm volatile("s_waitcnt vmcnt(" #n ")" ::: "memory")
#define BAR() __builtin_amdgcn_s_barrier()

__global__ __launch_bounds__(512) void k_prep(const u16* __restrict__ x, const u16* __restrict__ c,
                                              const u16* __restrict__ wmod, char* __restrict__ ws) {
  __shared__ __attribute__((aligned(16))) u16 lds[D * 8 + 64];
  const int tid = threadIdx.x, lane = tid & 63, w = tid >> 6;
  const int b = blockIdx.x;
  STAMP(0, 0);
  if (b < 256) {
    const int row = b * 8 + w;
    u32x4 v = *(const u32x4*)(x + (size_t)row * D + lane * 8);
    float f[8];
#pragma unroll
    for (int i = 0; i < 4; ++i) { f[2 * i] = bflo(v[i]); f[2 * i + 1] = bfhi(v[i]); }
    float s = 0;
#pragma unroll
    for (int i = 0; i < 8; ++i) s += f[i];
    const float m = wave_sum(s) * (1.f / D);
    float q = 0;
#pragma unroll
    for (int i = 0; i < 8; ++i) { f[i] -= m; q += f[i] * f[i]; }
    const float rs = rsqrtf(wave_sum(q) * (1.f / D) + EPS);
#pragma unroll
    for (int i = 0; i < 8; ++i) lds[(lane * 8 + i) * 8 + w] = (u16)(pk2(f[i] * rs, 0.f) & 0xffff);
    if (tid < 24) ((float*)(ws + OFF_ST))[(tid >> 3) * S + b * 8 + (tid & 7)] = 0.f;
    __syncthreads();
    u32x4 o = *(const u32x4*)(lds + tid * 8);
    *(u32x4*)(ws + OFF_NB + ((size_t)b * D + tid) * 16) = o;
  } else {
    const int j = b - 256;
    const int cs = j % 48, ks = j / 48;
    const int cg = lane & 7;
    const int rg = tid >> 3;
    float acc[8];
#pragma unroll
    for (int i = 0; i < 8; ++i) acc[i] = 0.f;
    u32x4 wv[2];
    float sc[2];
#pragma unroll
    for (int r = 0; r < 2; ++r) {
      const int k = ks * 128 + r * 64 + rg;
      wv[r] = *(const u32x4*)(wmod + (size_t)k * 3072 + cs * 64 + cg * 8);
      const float cv = bf2f(c[k]);
      sc[r] = rbf(cv / (1.f + __expf(-cv)));
    }
#pragma unroll
    for (int r = 0; r < 2; ++r)
#pragma unroll
      for (int i = 0; i < 4; ++i) { acc[2 * i] += sc[r] * bflo(wv[r][i]); acc[2 * i + 1] += sc[r] * bfhi(wv[r][i]); }
#pragma unroll
    for (int i = 0; i < 8; ++i) {
      acc[i] += __shfl_xor(acc[i], 8); acc[i] += __shfl_xor(acc[i], 16); acc[i] += __shfl_xor(acc[i], 32);
    }
    float* red = (float*)lds;
    if (lane < 8) {
#pragma unroll
      for (int i = 0; i < 8; ++i) red[w * 64 + lane * 8 + i] = acc[i];
    }
    __syncthreads();
    if (tid < 64) {
      float s = 0;
#pragma unroll
      for (int i = 0; i < 8; ++i) s += red[i * 64 + tid];
      ((float*)(ws + OFF_MODP))[ks * 3072 + cs * 64 + tid] = s;
    }
  }
  STAMP(0, 1);
}

__device__ __forceinline__ void glds16(const void* g, void* l) {
  __builtin_amdgcn_global_load_lds((const __attribute__((address_space(1))) unsigned*)g,
                                   (__attribute__((address_space(3))) unsigned*)l, 16, 0, 0);
}

constexpr int K2_ST = 6, K2_STB = 128 * 128 + 8 * 64 * 16;
__global__ __launch_bounds__(256) void k_gemm1(const u16* __restrict__ wre, const u16* __restrict__ wim,
                                               const u16* __restrict__ dre, const u16* __restrict__ dim_,
                                               char* __restrict__ ws) {
  extern __shared__ __attribute__((aligned(16))) char smem[];
  const int tid = threadIdx.x, lane = tid & 63;
  const int w = __builtin_amdgcn_readfirstlane(tid >> 6);
  const int b = blockIdx.x;
  STAMP(1, 0);
  const int xcd = b & 7, loc = b >> 3;
  const int mt = xcd * 4 + (loc >> 3);
  const int nt = loc & 7;
  const bool is_im = mt >= 16;
  const u16* W = (is_im ? wim : wre) + (size_t)(mt & 15) * 128 * S;
  const char* X = ws + OFF_NB + (size_t)nt * 64 * 16;
  const int urow = b * 4 + w;
  const u32x4 dv = *(const u32x4*)((urow < 512 ? dre : dim_) + (size_t)(urow & 511) * D + lane * 8);
  const float* modp = (const float*)(ws + OFF_MODP);
  float us = 0;
#pragma unroll
  for (int i = 0; i < 4; ++i)
    us += modv(modp, lane * 8 + 2 * i) * bflo(dv[i]) + modv(modp, lane * 8 + 2 * i + 1) * bfhi(dv[i]);
  us = wave_sum(us);
  if (lane == 0) ((float*)(ws + OFF_U))[urow] = us;
  const int wt = w >> 1, wd = w & 1;
  float c1[2][4];
#pragma unroll
  for (int df = 0; df < 2; ++df)
#pragma unroll
    for (int j = 0; j < 4; ++j) {
      float v = rbf(1.f + modv(modp, 512 + nt * 64 + wd * 32 + df * 16 + (lane >> 4) * 4 + j));
      c1[df][j] = is_im ? -v : v;
    }

  const u16* gw[4];
#pragma unroll
  for (int i = 0; i < 4; ++i) {
    const int row = (w * 4 + i) * 8 + (lane >> 3);
    const int cch = (lane & 7) ^ ((row >> 1) & 7);
    gw[i] = W + (size_t)row * S + cch * 8;
  }
  const char* gx[2];
#pragma unroll
  for (int i = 0; i < 2; ++i) gx[i] = X + (size_t)(w * 2 + i) * D * 16 + lane * 16;
  auto stage = [&](int kt, int slot) {
    char* sb = smem + slot * K2_STB;
#pragma unroll
    for (int i = 0; i < 4; ++i) glds16(gw[i] + kt * 64, sb + (w * 4 + i) * 1024);
#pragma unroll
    for (int i = 0; i < 2; ++i) glds16(gx[i] + (size_t)kt * 8 * D * 16, sb + 16384 + (w * 2 + i) * 1024);
  };
  f32x4 acc[2][4], rs[4];
#pragma unroll
  for (int i = 0; i < 2; ++i)
#pragma unroll
    for (int j = 0; j < 4; ++j) acc[i][j] = f32x4{0, 0, 0, 0};
#pragma unroll
  for (int j = 0; j < 4; ++j) rs[j] = f32x4{0, 0, 0, 0};
  const bf16x8 ones = {0x3f80, 0x3f80, 0x3f80, 0x3f80, 0x3f80, 0x3f80, 0x3f80, 0x3f80};
  const int fr = lane & 15, fq = lane >> 4;
  int offW[2];
#pragma unroll
  for (int ks = 0; ks < 2; ++ks) offW[ks] = (wt * 64 + fr) * 128 + (((ks * 4 + fq) ^ ((fr >> 1) & 7)) * 16);
  const int offX = 16384 + fq * 1024 + (wd * 32 + fr) * 16;
  constexpr int NT = S / 64;
  STAMP(1, 1);
  WAIT_V(0);
#pragma unroll
  for (int p = 0; p < K2_ST - 1; ++p) stage(p, p);
  int slot = 0;
  for (int kt = 0; kt < NT; ++kt) {
    if (kt + K2_ST - 1 <= NT) { WAIT_V(24); } else { WAIT_V(0); }
    BAR();
    if (kt + K2_ST - 1 < NT) { int ns = slot - 1; if (ns < 0) ns += K2_ST; stage(kt + K2_ST - 1, ns); }
    const char* sb = smem + slot * K2_STB;
#pragma unroll
    for (int ks = 0; ks < 2; ++ks) {
      bf16x8 wf[4], xf[2];
#pragma unroll
      for (int j = 0; j < 4; ++j) wf[j] = *(const bf16x8*)(sb + offW[ks] + j * 16 * 128);
#pragma unroll
      for (int i = 0; i < 2; ++i) xf[i] = *(const bf16x8*)(sb + offX + ks * 4096 + i * 256);
#pragma unroll
      for (int i = 0; i < 2; ++i)
#pragma unroll
        for (int j = 0; j < 4; ++j) acc[i][j] = __builtin_amdgcn_mfma_f32_16x16x32_bf16(xf[i], wf[j], acc[i][j], 0, 0, 0);
      if ((kt >> 2) == nt && wd == 0) {
#pragma unroll
        for (int j = 0; j < 4; ++j) rs[j] = __builtin_amdgcn_mfma_f32_16x16x32_bf16(ones, wf[j], rs[j], 0, 0, 0);
      }
    }
    ++slot; if (slot == K2_ST) slot = 0;
  }
  STAMP(1, 2);
  const int t0 = (mt & 15) * 128 + wt * 64;
#pragma unroll
  for (int i = 0; i < 2; ++i) {
    const int d = nt * 64 + wd * 32 + i * 16 + fq * 4;
    const int kblk = ((is_im ? 512 : 0) + d) >> 3;
#pragma unroll
    for (int j = 0; j < 4; ++j) {
      u32x2 o;
      o[0] = pk2(acc[i][j][0] * c1[i][0], acc[i][j][1] * c1[i][1]);
      o[1] = pk2(acc[i][j][2] * c1[i][2], acc[i][j][3] * c1[i][3]);
      *(u32x2*)(ws + OFF_PB + ((size_t)kblk * S + t0 + j * 16 + fr) * 16 + (fq & 1) * 8) = o;
    }
  }
  if (wd == 0 && fq == 0) {
#pragma unroll
    for (int j = 0; j < 4; ++j) ((float*)(ws + OFF_RSP))[nt * 4096 + mt * 128 + wt * 64 + j * 16 + fr] = rs[j][0];
  }
  STAMP(1, 3);
}

constexpr int K3_ST = 8, K3_STB = 64 * 128 + 8 * 64 * 16;
__global__ __launch_bounds__(256) void k_gemm2(const u16* __restrict__ dre, const u16* __restrict__ dim_,
                                               const u16* __restrict__ x, const u16* __restrict__ wr,
                                               char* __restrict__ ws) {
  extern __shared__ __attribute__((aligned(16))) char smem[];
  const int tid = threadIdx.x, lane = tid & 63;
  const int w = __builtin_amdgcn_readfirstlane(tid >> 6);
  const int b = blockIdx.x;
  STAMP(2, 0);
  const int xcd = b & 7, loc = b >> 3;
  const int tt = xcd * 4 + (loc >> 3);
  const int et = loc & 7;
  const int we = w >> 1, wt = w & 1;
  const int fr = lane & 15, fq = lane >> 4;
  const float* modp = (const float*)(ws + OFF_MODP);
  float ga[2][4], vv[2][4], ur[2][4], ui[2][4], rr[2], ri[2];
  u32x2 xv[2][2];
#pragma unroll
  for (int i = 0; i < 2; ++i)
#pragma unroll
    for (int j = 0; j < 4; ++j) {
      const int e = et * 64 + we * 32 + i * 16 + fq * 4 + j;
      ga[i][j] = modv(modp, 1024 + e);
      vv[i][j] = rbf(1.f + modv(modp, 4 * 512 + e)) * bf2f(wr[e]);
      ur[i][j] = ((const float*)(ws + OFF_U))[e];
      ui[i][j] = ((const float*)(ws + OFF_U))[512 + e];
    }
#pragma unroll
  for (int j = 0; j < 2; ++j) {
    const int t = tt * 64 + wt * 32 + j * 16 + fr;
    float a = 0, c = 0;
#pragma unroll
    for (int p = 0; p < 8; ++p) { a += ((const float*)(ws + OFF_RSP))[p * 4096 + t]; c += ((const float*)(ws + OFF_RSP))[p * 4096 + 2048 + t]; }
    rr[j] = a; ri[j] = c;
#pragma unroll
    for (int i = 0; i < 2; ++i) xv[i][j] = *(const u32x2*)(x + (size_t)t * D + et * 64 + we * 32 + i * 16 + fq * 4);
  }
  const u16* gw[2];
  size_t imoff = dim_ - dre;
#pragma unroll
  for (int i = 0; i < 2; ++i) {
    const int row = (w * 2 + i) * 8 + (lane >> 3);
    const int cch = (lane & 7) ^ ((row >> 1) & 7);
    gw[i] = dre + (size_t)(et * 64 + row) * D + cch * 8;
  }
  const char* gx[2];
#pragma unroll
  for (int i = 0; i < 2; ++i) gx[i] = ws + OFF_PB + ((size_t)(w * 2 + i) * S + tt * 64 + lane) * 16;
  auto stage = [&](int kt, int slot) {
    char* sb = smem + slot * K3_STB;
    const size_t wo = (kt >= 8 ? imoff : 0) + (kt & 7) * 64;
#pragma unroll
    for (int i = 0; i < 2; ++i) glds16(gw[i] + wo, sb + (w * 2 + i) * 1024);
#pragma unroll
    for (int i = 0; i < 2; ++i) glds16(gx[i] + (size_t)kt * 8 * S * 16, sb + 8192 + (w * 2 + i) * 1024);
  };
  f32x4 acc[2][2];
#pragma unroll
  for (int i = 0; i < 2; ++i)
#pragma unroll
    for (int j = 0; j < 2; ++j) acc[i][j] = f32x4{0, 0, 0, 0};
  int offW[2];
#pragma unroll
  for (int ks = 0; ks < 2; ++ks) offW[ks] = (we * 32 + fr) * 128 + (((ks * 4 + fq) ^ ((fr >> 1) & 7)) * 16);
  const int offX = 8192 + fq * 1024 + (wt * 32 + fr) * 16;
  constexpr int NT = 16;
  STAMP(2, 1);
  WAIT_V(0);
#pragma unroll
  for (int p = 0; p < K3_ST - 1; ++p) stage(p, p);
  int slot = 0;
  for (int kt = 0; kt < NT; ++kt) {
    if (kt + K3_ST - 1 <= NT) { WAIT_V(24); } else { WAIT_V(0); }
    BAR();
    if (kt + K3_ST - 1 < NT) { int ns = slot - 1; if (ns < 0) ns += K3_ST; stage(kt + K3_ST - 1, ns); }
    const char* sb = smem + slot * K3_STB;
#pragma unroll
    for (int ks = 0; ks < 2; ++ks) {
      bf16x8 wf[2], xf[2];
#pragma unroll
      for (int i = 0; i < 2; ++i) wf[i] = *(const bf16x8*)(sb + offW[ks] + i * 16 * 128);
#pragma unroll
      for (int j = 0; j < 2; ++j) xf[j] = *(const bf16x8*)(sb + offX + ks * 4096 + j * 256);
#pragma unroll
      for (int i = 0; i < 2; ++i)
#pragma unroll
        for (int j = 0; j < 2; ++j) acc[i][j] = __builtin_amdgcn_mfma_f32_16x16x32_bf16(wf[i], xf[j], acc[i][j], 0, 0, 0);
    }
    ++slot; if (slot == K3_ST) slot = 0;
  }
  STAMP(2, 2);
  float* st = (float*)(ws + OFF_ST);
#pragma unroll
  for (int j = 0; j < 2; ++j) {
    const int t = tt * 64 + wt * 32 + j * 16 + fr;
    float s0 = 0, s1 = 0, s2 = 0;
#pragma unroll
    for (int i = 0; i < 2; ++i) {
      float xx[4] = {bflo(xv[i][j][0]), bfhi(xv[i][j][0]), bflo(xv[i][j][1]), bfhi(xv[i][j][1])};
      float o[4];
#pragma unroll
      for (int q = 0; q < 4; ++q) {
        const float y = acc[i][j][q] + rr[j] * ur[i][q] - ri[j] * ui[i][q];
        o[q] = rbf(xx[q] + ga[i][q] * y);
        s0 += o[q]; s1 += o[q] * o[q]; s2 += o[q] * vv[i][q];
      }
      u32x2 ov; ov[0] = pk2(o[0], o[1]); ov[1] = pk2(o[2], o[3]);
      *(u32x2*)(ws + OFF_X1 + ((size_t)t * D + et * 64 + we * 32 + i * 16 + fq * 4) * 2) = ov;
    }
    s0 += __shfl_xor(s0, 16); s0 += __shfl_xor(s0, 32);
    s1 += __shfl_xor(s1, 16); s1 += __shfl_xor(s1, 32);
    s2 += __shfl_xor(s2, 16); s2 += __shfl_xor(s2, 32);
    if (fq < 3) atomicAdd(st + fq * S + t, fq == 0 ? s0 : (fq == 1 ? s1 : s2));
  }
  STAMP(2, 3);
}

struct Top2 { float v0, v1; int i0, i1; };
__device__ __forceinline__ bool better(float a, int ia, float b, int ib) { return a > b || (a == b && ia < ib); }
__device__ __forceinline__ void ins(Top2& t, float v, int i) {
  if (better(v, i, t.v0, t.i0)) { t.v1 = t.v0; t.i1 = t.i0; t.v0 = v; t.i0 = i; }
  else if (better(v, i, t.v1, t.i1)) { t.v1 = v; t.i1 = i; }
}
__global__ __launch_bounds__(256) void k_route(const u16* __restrict__ wr, const u16* __restrict__ wg,
                                               const u16* __restrict__ wu, char* __restrict__ ws, u16* __restrict__ out) {
  __shared__ float red[4 * 32 + 16];
  __shared__ int redi[8];
  const int tid = threadIdx.x, lane = tid & 63, w = tid >> 6;
  const int b = blockIdx.x;
  const float* modp = (const float*)(ws + OFF_MODP);
  const float* st = (const float*)(ws + OFF_ST);
  const u16* x1 = (const u16*)(ws + OFF_X1);
  STAMP(3, 0);
  const bool moe = (b >> 3) < 28;
  const int chunk = (b & 7) * 28 + (b >> 3);
  u32x4 gv[2], uv[2];
  if (moe) {
#pragma unroll
    for (int r = 0; r < 2; ++r) {
      gv[r] = *(const u32x4*)(wg + (size_t)(tid + r * 256) * F + chunk * 8);
      uv[r] = *(const u32x4*)(wu + (size_t)(tid + r * 256) * F + chunk * 8);
    }
  }
  float cb1[2], sb[2];
  float V1 = 0, V0 = 0;
#pragma unroll
  for (int r = 0; r < 2; ++r) {
    const int e = tid + r * 256;
    cb1[r] = rbf(1.f + modv(modp, 4 * 512 + e)); sb[r] = modv(modp, 3 * 512 + e);
    const float wv = bf2f(wr[e]);
    V1 += cb1[r] * wv; V0 += sb[r] * wv;
  }
  V1 = wave_sum(V1); V0 = wave_sum(V0);
  if (lane == 0) { red[w] = V1; red[4 + w] = V0; }
  __syncthreads();
  V1 = (red[0] + red[1]) + (red[2] + red[3]); V0 = (red[4] + red[5]) + (red[6] + red[7]);
  __syncthreads();
  Top2 t{-INFINITY, -INFINITY, 0x7fffffff, 0x7fffffff};
#pragma unroll
  for (int h = 0; h < 2; ++h) {
    const int r0 = (h * 256 + tid) * 4;
    f32x4 a = *(const f32x4*)(st + r0), q = *(const f32x4*)(st + S + r0), d = *(const f32x4*)(st + 2 * S + r0);
#pragma unroll
    for (int i = 0; i < 4; ++i) {
      const float m = a[i] * (1.f / D);
      const float var = fmaxf(q[i] * (1.f / D) - m * m, 0.f);
      const float rs = rsqrtf(var + EPS);
      ins(t, rs * (d[i] - m * V1) + V0, r0 + i);
    }
  }
#pragma unroll
  for (int o = 32; o >= 1; o >>= 1) {
    float v0 = __shfl_xor(t.v0, o), v1 = __shfl_xor(t.v1, o);
    int i0 = __shfl_xor(t.i0, o), i1 = __shfl_xor(t.i1, o);
    ins(t, v0, i0); ins(t, v1, i1);
  }
  if (lane == 0) { red[2 * w] = t.v0; red[2 * w + 1] = t.v1; redi[2 * w] = t.i0; redi[2 * w + 1] = t.i1; }
  __syncthreads();
  t = Top2{-INFINITY, -INFINITY, 0x7fffffff, 0x7fffffff};
#pragma unroll
  for (int i = 0; i < 8; ++i) ins(t, red[i], redi[i]);
  __syncthreads();
  STAMP(3, 1);
  t.i0 = min(t.i0, S - 1); t.i1 = min(t.i1, S - 1);
  if (b == 0 && tid == 0) {
    int* sel = (int*)(ws + OFF_SEL);
    sel[0] = t.i0; sel[1] = t.i1;
    ((float*)sel)[2] = t.v0; ((float*)sel)[3] = t.v1;
  }
  {
    float c8[8], s8[8], g8[8];
#pragma unroll
    for (int i = 0; i < 8; ++i) {
      const int e = lane * 8 + i;
      c8[i] = rbf(1.f + modv(modp, 4 * 512 + e)); s8[i] = modv(modp, 3 * 512 + e); g8[i] = modv(modp, 5 * 512 + e);
    }
#pragma unroll
    for (int r = 0; r < 2; ++r) {
      const int row = b * 8 + w * 2 + r;
      const float m = st[row] * (1.f / D);
      const float rs = rsqrtf(fmaxf(st[S + row] * (1.f / D) - m * m, 0.f) + EPS);
      u32x4 v = *(const u32x4*)(x1 + (size_t)row * D + lane * 8), o;
#pragma unroll
      for (int i = 0; i < 4; ++i) {
        const float a = bflo(v[i]), c = bfhi(v[i]);
        o[i] = pk2(a + g8[2 * i] * ((a - m) * rs * c8[2 * i] + s8[2 * i]),
                   c + g8[2 * i + 1] * ((c - m) * rs * c8[2 * i + 1] + s8[2 * i + 1]));
      }
      if (row != t.i0 && row != t.i1) *(u32x4*)(out + (size_t)row * D + lane * 8) = o;
    }
  }
  STAMP(3, 2);
  if (moe) {
    float acc[32];
#pragma unroll
    for (int i = 0; i < 32; ++i) acc[i] = 0.f;
#pragma unroll
    for (int k = 0; k < 2; ++k) {
      const int row = k ? t.i1 : t.i0;
      const float m = st[row] * (1.f / D);
      const float rs = rsqrtf(fmaxf(st[S + row] * (1.f / D) - m * m, 0.f) + EPS);
#pragma unroll
      for (int r = 0; r < 2; ++r) {
        const float h = rbf((bf2f(x1[(size_t)row * D + tid + r * 256]) - m) * rs * cb1[r] + sb[r]);
#pragma unroll
        for (int i = 0; i < 4; ++i) {
          acc[k * 16 + 2 * i] += h * bflo(gv[r][i]); acc[k * 16 + 2 * i + 1] += h * bfhi(gv[r][i]);
          acc[k * 16 + 8 + 2 * i] += h * bflo(uv[r][i]); acc[k * 16 + 8 + 2 * i + 1] += h * bfhi(uv[r][i]);
        }
      }
    }
#pragma unroll
    for (int i = 0; i < 32; ++i) acc[i] = wave_sum(acc[i]);
    if (lane == 0) {
#pragma unroll
      for (int i = 0; i < 32; ++i) red[w * 32 + i] = acc[i];
    }
    __syncthreads();
    if (tid < 16) {
      const int k = tid >> 3, i = tid & 7;
      float zg = 0, zu = 0;
#pragma unroll
      for (int q = 0; q < 4; ++q) { zg += red[q * 32 + k * 16 + i]; zu += red[q * 32 + k * 16 + 8 + i]; }
      zg = rbf(zg); zu = rbf(zu);
      const float g = rbf(zg / (1.f + __expf(-zg)));
      ((float*)(ws + OFF_HID))[k * F + chunk * 8 + i] = rbf(g * zu);
    }
  }
  STAMP(3, 3);
}

__global__ __launch_bounds__(256) void k_down(const u16* __restrict__ wd, char* __restrict__ ws, u16* __restrict__ out) {
  __shared__ float red[4 * 16];
  const int tid = threadIdx.x, lane = tid & 63, w = tid >> 6;
  const int b = blockIdx.x;
  STAMP(4, 0);
  const int chunk = (b & 7) * 8 + (b >> 3);
  const float* hid = (const float*)(ws + OFF_HID);
  u32x4 v[7];
#pragma unroll
  for (int r = 0; r < 7; ++r) v[r] = *(const u32x4*)(wd + (size_t)(tid + r * 256) * D + chunk * 8);
  float acc[16];
#pragma unroll
  for (int i = 0; i < 16; ++i) acc[i] = 0.f;
#pragma unroll
  for (int r = 0; r < 7; ++r) {
    const float h0 = hid[tid + r * 256], h1 = hid[F + tid + r * 256];
#pragma unroll
    for (int i = 0; i < 4; ++i) {
      acc[2 * i] += h0 * bflo(v[r][i]); acc[2 * i + 1] += h0 * bfhi(v[r][i]);
      acc[8 + 2 * i] += h1 * bflo(v[r][i]); acc[8 + 2 * i + 1] += h1 * bfhi(v[r][i]);
    }
  }
#pragma unroll
  for (int i = 0; i < 16; ++i) acc[i] = wave_sum(acc[i]);
  if (lane == 0) {
#pragma unroll
    for (int i = 0; i < 16; ++i) red[w * 16 + i] = acc[i];
  }
  __syncthreads();
  STAMP(4, 1);
  if (tid < 16) {
    const int k = tid >> 3, e = chunk * 8 + (tid & 7);
    const int* sel = (const int*)(ws + OFF_SEL);
    const int row = sel[k];
    const float rv = rbf(((const float*)sel)[2 + k]);
    const float sg = rbf(1.f / (1.f + __expf(-rv)));
    float y = rbf((red[tid] + red[16 + tid]) + (red[32 + tid] + red[48 + tid]));
    y = rbf(y * sg);
    const float* modp = (const float*)(ws + OFF_MODP);
    const float* st = (const float*)(ws + OFF_ST);
    const float m = st[row] * (1.f / D);
    const float rs = rsqrtf(fmaxf(st[S + row] * (1.f / D) - m * m, 0.f) + EPS);
    const float a = bf2f(((const u16*)(ws + OFF_X1))[(size_t)row * D + e]);
    const float h = rbf((a - m) * rs * rbf(1.f + modv(modp, 4 * 512 + e)) + modv(modp, 3 * 512 + e));
    const float o = a + rbf(modv(modp, 5 * 512 + e) * rbf(h + y));
    out[(size_t)row * D + e] = (u16)(pk2(o, 0.f) & 0xffff);
  }
  STAMP(4, 2);
}

template<int ROT,int AUX>
__global__ __launch_bounds__(512) void k_probe(const char* __restrict__ sre, const char* __restrict__ sim, char* ws){
  extern __shared__ __attribute__((aligned(16))) char smem[];
  const int lane=threadIdx.x&63; const int w=__builtin_amdgcn_readfirstlane(threadIdx.x>>6);
  const int b=blockIdx.x; const int mt=(b&7)*4+(b>>6); const int nt=(b>>3)&7;
  const char* base=(mt<16?sre:sim)+(size_t)(mt&15)*128*4096+(size_t)(w&3)*32*4096+(w>>2)*128+(lane>>3)*4096+(lane&7)*16;
  unsigned long long r0=__builtin_amdgcn_s_memrealtime();
  #pragma unroll
  for(int j=0;j<16;++j){
    const int jj = (j + nt*ROT) & 15;
    #pragma unroll
    for(int p=0;p<4;++p)
      __builtin_amdgcn_global_load_lds((const __attribute__((address_space(1))) unsigned*)(base+p*8*4096+jj*256),(__attribute__((address_space(3))) unsigned*)(smem+w*16384+((j*4+p)&15)*1024),16,0,AUX);
    asm volatile("s_waitcnt vmcnt(12)":::"memory");
  }
  asm volatile("s_waitcnt vmcnt(0)":::"memory");
  __syncthreads();
  unsigned long long r1=__builtin_amdgcn_s_memrealtime();
  if(threadIdx.x==0) ((unsigned long long*)(ws + (128u<<20)))[b] = r1-r0;
}
__global__ void k_leak(char* ws) {
  const unsigned long long* sb = (const unsigned long long*)(ws + (128u<<20));
  unsigned long long t1 = 0;
  for (int b = threadIdx.x; b < 256; b += 64) t1 += sb[b];
  for (int o = 32; o >= 1; o >>= 1) t1 += __shfl_xor(t1, o);
  unsigned long long d = LEAK_ON * t1 / 8; if (d > 200000) d = 200000;
  unsigned long long s0 = __builtin_amdgcn_s_memrealtime();
  while (__builtin_amdgcn_s_memrealtime() - s0 < d) __builtin_amdgcn_s_sleep(1);
}
extern "C" void kernel_launch(void* const* d_in, const int* in_sizes, int n_in, void* d_out, int out_size, void* d_ws,
                              size_t ws_size, hipStream_t stream) {
  const u16* x = (const u16*)d_in[0];
  const u16* sre = (const u16*)d_in[1];
  const u16* sim = (const u16*)d_in[2];
  const u16* dre = (const u16*)d_in[3];
  const u16* dim_ = (const u16*)d_in[4];
  const u16* wr = (const u16*)d_in[6];
  const u16* wg = (const u16*)d_in[7];
  const u16* wu = (const u16*)d_in[8];
  const u16* wd = (const u16*)d_in[9];
  const u16* c = (const u16*)d_in[10];
  const u16* wmod = (const u16*)d_in[11];
  char* ws = (char*)d_ws;
  u16* out = (u16*)d_out;
  hipFuncSetAttribute((const void*)k_gemm1, hipFuncAttributeMaxDynamicSharedMemorySize, K2_ST * K2_STB);
  hipFuncSetAttribute((const void*)k_gemm2, hipFuncAttributeMaxDynamicSharedMemorySize, K3_ST * K3_STB);
  hipFuncSetAttribute((const void*)k_probe<PROT,PAUX>, hipFuncAttributeMaxDynamicSharedMemorySize, 131072);
  k_probe<PROT,PAUX><<<256,512,131072,stream>>>((const char*)sre,(const char*)sim,ws);
  k_prep<<<256 + 192, 512, 0, stream>>>(x, c, wmod, ws);
  k_gemm1<<<256, 256, K2_ST * K2_STB, stream>>>(sre, sim, dre, dim_, ws);
  k_gemm2<<<256, 256, K3_ST * K3_STB, stream>>>(dre, dim_, x, wr, ws);
  k_route<<<256, 256, 0, stream>>>(wr, wg, wu, ws, out);
  k_down<<<64, 256, 0, stream>>>(wd, ws, out);
  k_leak<<<1, 64, 0, stream>>>(ws);
}
```

```cpp
#include <hip/hip_runtime.h>
#include <hip/hip_bf16.h>
#include <stdint.h>

typedef short bf16x8 __attribute__((ext_vector_type(8)));
typedef short s16x4 __attribute__((ext_vector_type(4)));
typedef float f32x4 __attribute__((ext_vector_type(4)));
typedef float f32x2 __attribute__((ext_vector_type(2)));
typedef unsigned u32x4 __attribute__((ext_vector_type(4)));
typedef unsigned u32x2 __attribute__((ext_vector_type(2)));
typedef __bf16 bf16x2_t __attribute__((ext_vector_type(2)));
typedef unsigned short u16;
#define LAS __attribute__((address_space(3)))
#define GAS __attribute__((address_space(1)))

constexpr int S = 2048, D = 512, F = 1792;
constexpr float EPS = 1e-5f;
constexpr int SC1 = 16;
constexpr unsigned SPIN_CAP = 1u << 16;

#define FS1 64
#define ROTX (cc * 8 + xcd)
#define DLY2 8
#define DLYA 6
#define DLYB 8
#define DLYC 8

constexpr unsigned OFF_EPOCH = 0;
constexpr unsigned OFF_F1 = 4096;
constexpr unsigned OFF_F2 = OFF_F1 + 8 * 256 * 128;
constexpr unsigned OFF_CAND = OFF_F2 + 256 * 128;
constexpr unsigned OFF_HID = OFF_CAND + 256 * 128;
constexpr unsigned OFF_MU = OFF_HID + 4 * 16384;
constexpr unsigned OFF_STG = OFF_MU + 48 * 1024;
constexpr unsigned OFF_ROWH = OFF_STG + 256 * 1024;
constexpr unsigned OFF_ROWX = OFF_ROWH + 64 * 2048;
constexpr unsigned OFF_NB = 2u << 20;
constexpr unsigned OFF_PB = 4u << 20;
constexpr unsigned OFF_RT = 8u << 20;
constexpr unsigned OFF_RD = 12u << 20;
static_assert(OFF_ROWX + 64 * 2048 <= OFF_NB, "ws map");

constexpr int L_TR = 0;
constexpr int L_RED = 8192;
constexpr int L_MOD = 10240;
constexpr int L_RS = 12288;
constexpr int L_V = 12800;
constexpr int L_Y = 13056;
constexpr int L_SLOT = 16384;
constexpr int LDS_BYTES = L_SLOT + 9 * 16384;

__device__ __forceinline__ float bflo(unsigned w) { return __builtin_bit_cast(float, w << 16); }
__device__ __forceinline__ float bfhi(unsigned w) { return __builtin_bit_cast(float, w & 0xffff0000u); }
__device__ __forceinline__ unsigned pk2(float lo, float hi) {
  f32x2 v = {lo, hi};
  return __builtin_bit_cast(unsigned, __builtin_convertvector(v, bf16x2_t));
}
__device__ __forceinline__ float rbf(float x) { return bflo(pk2(x, 0.f)); }
__device__ __forceinline__ float sigm(float x) { return __builtin_amdgcn_rcpf(1.f + __expf(-x)); }
__device__ __forceinline__ unsigned fbits(float x) { return __builtin_bit_cast(unsigned, x); }
__device__ __forceinline__ float bitsf(unsigned x) { return __builtin_bit_cast(float, x); }
template <int C> __device__ __forceinline__ unsigned dppu(unsigned v) {
  return (unsigned)__builtin_amdgcn_update_dpp(0, (int)v, C, 0xf, 0xf, true);
}
template <int C> __device__ __forceinline__ float dppf(float v) { return bitsf(dppu<C>(fbits(v))); }
constexpr int DPP_X1 = 0xB1, DPP_X2 = 0x4E, DPP_HM = 0x141, DPP_M = 0x140, DPP_ROR8 = 0x128;
__device__ __forceinline__ float tr32(float a, float b) { auto r = __builtin_amdgcn_permlane32_swap(fbits(a), fbits(b), false, false); return bitsf(r[0]) + bitsf(r[1]); }
__device__ __forceinline__ float tr16(float a, float b) { auto r = __builtin_amdgcn_permlane16_swap(fbits(a), fbits(b), false, false); return bitsf(r[0]) + bitsf(r[1]); }
__device__ __forceinline__ float sum8lanes(float v) { v += dppf<DPP_X1>(v); v += dppf<DPP_X2>(v); v += dppf<DPP_HM>(v); return v; }
__device__ __forceinline__ float wave_sum(float v) { v = sum8lanes(v); v += dppf<DPP_M>(v); v = tr16(v, v); return tr32(v, v); }
__device__ __forceinline__ unsigned wave_max(unsigned v) {
  v = max(v, dppu<DPP_X1>(v)); v = max(v, dppu<DPP_X2>(v)); v = max(v, dppu<DPP_HM>(v)); v = max(v, dppu<DPP_M>(v));
  auto r = __builtin_amdgcn_permlane16_swap(v, v, false, false); v = max(r[0], r[1]);
  auto q = __builtin_amdgcn_permlane32_swap(v, v, false, false); return max(q[0], q[1]);
}

#define WAITV(n) asm volatile("s_waitcnt vmcnt(%0)" ::"i"(n) : "memory")
#define BAR() asm volatile("s_waitcnt lgkmcnt(0)\n\ts_barrier" ::: "memory")
#define RAWBAR() asm volatile("s_barrier" ::: "memory")
#define SCHED() __builtin_amdgcn_sched_barrier(0)
#define TIE1(a) asm volatile("" : "+v"(a)::"memory")
#define TIE2(a, b) asm volatile("" : "+v"(a), "+v"(b)::"memory")
#define TIE4(a, b, c, d) asm volatile("" : "+v"(a), "+v"(b), "+v"(c), "+v"(d)::"memory")
#define WAITV1(n, a) asm volatile("s_waitcnt vmcnt(%1)" : "+v"(a) : "i"(n) : "memory")

template <int IMM> __device__ __forceinline__ void glds(const void* sbase, unsigned voff, unsigned ldsaddr) {
  asm volatile("s_mov_b32 m0, %2\n\ts_nop 0\n\tglobal_load_lds_dwordx4 %0, %1 offset:%3"
               ::"v"(voff), "s"(sbase), "s"(ldsaddr - IMM), "i"(IMM) : "memory");
}
template <int LOFF, int X> __device__ __forceinline__ void glds2(const void* sbase, unsigned voff0, unsigned voff1, unsigned ldsbase) {
  unsigned t0, t1;
  asm volatile("s_add_u32 m0, %5, %6\n\tv_xor_b32 %0, %7, %2\n\tv_xor_b32 %1, %7, %3\n\t"
               "global_load_lds_dwordx4 %0, %4\n\tglobal_load_lds_dwordx4 %1, %4 offset:1024"
               : "=&v"(t0), "=&v"(t1) : "v"(voff0), "v"(voff1), "s"(sbase), "s"(ldsbase), "i"(LOFF), "i"(X) : "memory", "scc");
}
template <int LOFF, int X> __device__ __forceinline__ void glds2b(const void* sb0, const void* sb1, unsigned voff0, unsigned voff1, unsigned ldsbase) {
  unsigned t0, t1;
  asm volatile("s_add_u32 m0, %6, %7\n\tv_xor_b32 %0, %8, %2\n\tv_xor_b32 %1, %8, %3\n\t"
               "global_load_lds_dwordx4 %0, %4\n\tglobal_load_lds_dwordx4 %1, %5 offset:1024"
               : "=&v"(t0), "=&v"(t1) : "v"(voff0), "v"(voff1), "s"(sb0), "s"(sb1), "s"(ldsbase), "i"(LOFF), "i"(X) : "memory", "scc");
}
__device__ __forceinline__ float dot2(unsigned a, unsigned b, float c) {
  return __builtin_amdgcn_fdot2_f32_bf16(__builtin_bit_cast(bf16x2_t, a), __builtin_bit_cast(bf16x2_t, b), c, false);
}
template <int IMM> __device__ __forceinline__ void gld16(u32x4& d, const void* sbase, unsigned voff) {
  asm volatile("global_load_dwordx4 %0, %1, %2 offset:%3 sc1" : "=v"(d) : "v"(voff), "s"(sbase), "i"(IMM) : "memory");
}
__device__ __forceinline__ void gld8(u32x2& d, const void* sbase, unsigned voff) {
  asm volatile("global_load_dwordx2 %0, %1, %2 sc1" : "=v"(d) : "v"(voff), "s"(sbase) : "memory");
}
__device__ __forceinline__ void gld4(unsigned& d, const void* sbase, unsigned voff) {
  asm volatile("global_load_dword %0, %1, %2 sc1" : "=v"(d) : "v"(voff), "s"(sbase) : "memory");
}
__device__ __forceinline__ void pld16(u32x4& d, const void* sbase, unsigned voff) {
  asm volatile("global_load_dwordx4 %0, %1, %2" : "=v"(d) : "v"(voff), "s"(sbase) : "memory");
}
__device__ __forceinline__ void pld4(unsigned& d, const void* sbase, unsigned voff) {
  asm volatile("global_load_dword %0, %1, %2" : "=v"(d) : "v"(voff), "s"(sbase) : "memory");
}
__device__ __forceinline__ void gst16(u32x4 d, const void* sbase, unsigned voff) {
  asm volatile("global_store_dwordx4 %0, %1, %2 sc1\n\ts_nop 1" ::"v"(voff), "v"(d), "s"(sbase) : "memory");
}
__device__ __forceinline__ f32x4 mfma(u32x4 a, u32x4 b, f32x4 c) {
  return __builtin_amdgcn_mfma_f32_16x16x32_bf16(__builtin_bit_cast(bf16x8, a), __builtin_bit_cast(bf16x8, b), c, 0, 0, 0);
}
__device__ __forceinline__ void mfma_acc(f32x4& c, u32x4 a, u32x4 b) {
  asm volatile("v_mfma_f32_16x16x32_bf16 %0, %1, %2, %0" : "+v"(c) : "v"(a), "v"(b));
}
__device__ __forceinline__ void mfma_init(f32x4& c, u32x4 a, u32x4 b) {
  asm volatile("v_mfma_f32_16x16x32_bf16 %0, %1, %2, 0" : "=&v"(c) : "v"(a), "v"(b));
}
#define MFMA_DONE2(a, b) asm volatile("s_nop 7" : "+v"(a), "+v"(b))
__device__ __forceinline__ void poll16(u32x4& g, const void* sbase, unsigned voff, unsigned tag) {
  unsigned sp = 0;
  for (;;) {
    gld16<0>(g, sbase, voff); WAITV1(0, g);
    if (__all(g[3] == tag) || ++sp >= SPIN_CAP) break;
    __builtin_amdgcn_s_sleep(1);
  }
}

#define STAMP(j)

constexpr int XRING = 3, DRING = 26, WD_MID_N = 2, WD_LATE_N = 1, CODE_LINES = 126, YSLOT = 0;
#define DSLOT0(i) ((i) == 0 ? 0 : (i) == 1 ? 2 : (i) == 2 ? 4 : 5)
#define DSLOT1(i) ((i) == 0 ? 1 : (i) == 1 ? 3 : (i) == 2 ? 7 : 8)
struct Args {
  const u16 *x, *sre, *sim, *dre, *dim_, *wr, *wg, *wu, *wd, *c, *wmod;
  char* ws;
  u16* out;
};

__global__ __launch_bounds__(512, 2) void k_mega(Args a) {
  LAS char* const sm = reinterpret_cast<LAS char*>(static_cast<unsigned>(sizeof(char) - 1));
  constexpr unsigned lds0 = 0;
  STAMP(0);
  const int tid = threadIdx.x, lane = tid & 63;
  const int w = __builtin_amdgcn_readfirstlane(tid >> 6);
  const int b = blockIdx.x;
  char* ws = a.ws;

  u32x4 xv;
  unsigned ep, cw;
  pld16(xv, a.x, (unsigned)b * 8192 + tid * 16);
  pld4(ep, ws, 0u);
  {
    const char* pc;
    asm volatile("s_getpc_b64 %0" : "=s"(pc));
    pld4(cw, pc, (unsigned)min((b >> 3) * 8 + w, CODE_LINES - 1) * 128);
  }
  const int xcd = b & 7, q = (b >> 3) & 7, p = xcd * 4 + (b >> 6);
  const int cc = q >> 2, nt = q & 3;
  const int dq = w & 3, h = w >> 2;
  const int fr = lane & 15, fq = lane >> 4, ech = tid & 7, et = tid >> 3;
  const int rot = ROTX;
  const int tfo = dq ^ (2 * h);
  const char* Wp = (const char*)(cc ? a.sim : a.sre) + (size_t)(p * 64 + tfo * 16) * (S * 2) + h * 128;
  const unsigned swz0 = (ech ^ fq) * 16;
  const unsigned wv0 = (lane >> 3) * (S * 2) + rot * 256 + swz0, wv1 = wv0 ^ 64;
  const unsigned ldst = lds0 + L_SLOT + h * 8192 + tfo * 2048;
#define GW(j) glds2b<((j) & 7) * 16384, (j) * 256>(Wp, Wp + (8 * S * 2 - 1024), wv0, wv1, ldst)
  const int mcs = b % 48, mks = (b / 48) & 3;
  u32x4 mw0, mw1, wre;
  unsigned c01;
#define MOD_LD() do { const int mk = mks * 128 + et * 2; const unsigned mo = mk * 6144 + mcs * 128 + ech * 16; \
    pld16(mw0, a.wmod, mo); pld16(mw1, a.wmod + 3072, mo); pld4(c01, a.c, mk * 2); pld16(wre, a.wr, q * 128 + ech * 16); } while (0)
#define MOD_A() do { TIE4(mw0, mw1, wre, c01); \
    const float cv0 = bflo(c01), cv1 = bfhi(c01); \
    const float s0 = rbf(cv0 * sigm(cv0)), s1 = rbf(cv1 * sigm(cv1)); \
    float ac[8]; \
    _Pragma("unroll") for (int i = 0; i < 4; ++i) { \
      ac[2 * i] = s0 * bflo(mw0[i]) + s1 * bflo(mw1[i]); ac[2 * i + 1] = s0 * bfhi(mw0[i]) + s1 * bfhi(mw1[i]); } \
    float t4[4], t2[2]; \
    _Pragma("unroll") for (int i = 0; i < 4; ++i) t4[i] = tr32(ac[i], ac[i + 4]); \
    _Pragma("unroll") for (int i = 0; i < 2; ++i) { t2[i] = tr16(t4[i], t4[i + 2]); t2[i] += dppf<DPP_ROR8>(t2[i]); } \
    if (!(lane & 8)) *(LAS f32x2*)(sm + L_RED + w * 256 + (ech * 8 + (lane >> 5) * 4 + ((lane >> 4) & 1) * 2) * 4) = f32x2{t2[0], t2[1]}; } while (0)
#define MOD_B() do { if (w == 7 && b < 192) { \
    const f32x4 s = *(LAS f32x4*)(sm + L_RED + fq * 512 + fr * 16) + *(LAS f32x4*)(sm + L_RED + fq * 512 + 256 + fr * 16); \
    const float v = tr16(tr32(s[0], s[2]), tr32(s[1], s[3]));       \
    const unsigned gq = ((fbits(v) + 0x80u) & ~0xffu) | ((tag >> (8 * fq)) & 0xffu); \
    auto r0 = __builtin_amdgcn_permlane16_swap(gq, gq, false, false); \
    auto r1 = __builtin_amdgcn_permlane32_swap(r0[0], r0[0], false, false); \
    auto r2 = __builtin_amdgcn_permlane32_swap(r0[1], r0[1], false, false); \
    if (lane < 16) gst16(u32x4{r1[0], r2[0], r1[1], r2[1]}, ws + (OFF_MU + (mcs * 4 + mks) * 256), lane * 16); } } while (0)
  RAWBAR();
  MOD_LD();
  GW(0);
  WAITV(7); TIE2(xv, ep);
  STAMP(1);
  const unsigned tag = (unsigned)__builtin_amdgcn_readfirstlane(ep) + 1u;
  {
    float s1 = 0.f, s2 = 0.f;
#pragma unroll
    for (int k = 0; k < 4; ++k) { const unsigned t = xv[k]; s1 = dot2(t, 0x3f803f80u, s1); s2 = dot2(t, t, s2); }
    float v = tr32(s1, s2);
    v = sum8lanes(v); v += dppf<DPP_M>(v); v = tr16(v, v);
    const float m = bitsf(__builtin_amdgcn_readlane(fbits(v), 0)) * (1.f / D);
    const float rstd = __builtin_amdgcn_rsqf(fmaxf(bitsf(__builtin_amdgcn_readlane(fbits(v), 32)) * (1.f / D) - m * m, 0.f) + EPS);
    const float mb = -m * rstd;
    const unsigned tro = L_TR + lane * 128 + ech * 16 + w * 2;
#pragma unroll
    for (int i = 0; i < 4; ++i) {
      const unsigned pk = pk2(bflo(xv[i]) * rstd + mb, bfhi(xv[i]) * rstd + mb);
      *(LAS u16*)(sm + (tro ^ (i * 32))) = (u16)pk;
      *(LAS u16*)(sm + (tro ^ (i * 32 + 16))) = (u16)(pk >> 16);
    }
    BAR();
    gst16(*(LAS u32x4*)(sm + L_TR + et * 128 + ((ech ^ (et & 7)) * 16)), ws + (OFF_NB + b * 8192), tid * 16);
  }
  RAWBAR();
  GW(1);
  GW(2);
  WAITV(7); TIE1(cw);
  MOD_A();
  WAITV(4);
  BAR();
  STAMP(2);
  if (lane < FS1 / 16) gst16(u32x4{tag, tag, tag, tag}, ws + (OFF_F1 + (w * 256 + b) * FS1), lane * 16);
  RAWBAR();
  MOD_B();
  {
    const char* tp = (const char*)(w < 4 ? a.dre : a.dim_) + b * 2048 + (w & 3) * 512;
    unsigned d;
    asm volatile("s_load_dword %0, %1, 0x0\n\ts_load_dword %0, %1, 0x40\n\ts_load_dword %0, %1, 0x80\n\ts_load_dword %0, %1, 0xc0\n\t"
                 "s_load_dword %0, %1, 0x100\n\ts_load_dword %0, %1, 0x140\n\ts_load_dword %0, %1, 0x180\n\ts_load_dword %0, %1, 0x1c0"
                 : "=&s"(d) : "s"(tp) : "memory");
    const unsigned f1off = OFF_F1 + (xcd * 256 + w * 32 + (lane & 31)) * FS1;
    unsigned fl;
  GW(3);
  GW(4);
  GW(5);
  gld4(fl, ws, f1off);
  GW(6);
  GW(7);
  WAITV1(4, fl);
    asm volatile("s_waitcnt lgkmcnt(0)" : "+s"(d)::"memory");
    if (__builtin_expect(!__all(fl == tag), 0)) {
      unsigned sp = 0;
      do { __builtin_amdgcn_s_sleep(1); gld4(fl, ws, f1off); WAITV1(0, fl); } while (!__all(fl == tag) && ++sp < SPIN_CAP);
    }
  }
  RAWBAR();
  STAMP(3);

  const unsigned xvo = fq * 8192 + (nt * 128 + dq * 32 + fr) * 16 + h * 65536 + rot * 131072;
  u32x4 xr[XRING][4];
  unsigned xa;
#define LX(j, i) do { if ((i) == 0) { xa = xvo ^ ((j) * 131072); } gld16<((i) & 1) * 256>(xr[(j) % XRING][i], ws + OFF_NB + ((i) >> 1) * 32768, xa); } while (0)
  LX(0, 0);
  LX(0, 1);
  LX(0, 2);
  LX(0, 3);
  LX(1, 0);
  LX(1, 1);
  LX(1, 2);
  LX(1, 3);
  const unsigned fro = L_SLOT + fr * 128 + ((fq ^ (fr >> 1)) * 16);
  unsigned wra[2][4][2];
#pragma unroll
  for (int t = 0; t < 4; ++t) {
    wra[0][t][0] = fro + h * 8192 + (t ^ tfo) * 2048; wra[0][t][1] = wra[0][t][0] ^ 64;
    wra[1][t][0] = wra[0][t][0] + 65536; wra[1][t][1] = wra[0][t][1] + 65536;
    TIE4(wra[0][t][0], wra[0][t][1], wra[1][t][0], wra[1][t][1]);
  }
  u32x4 ones = {0x3f803f80u, 0x3f803f80u, 0x3f803f80u, 0x3f803f80u};
  f32x4 rsa = {0, 0, 0, 0}, acc[2][4];
  asm volatile("s_nop 1" : "+v"(ones), "+v"(rsa));
  u32x4 wf0[4], wf1[4];
  const char* DA = (const char*)a.dre + (size_t)(q * 64 + tfo * 16) * (D * 2) + h * 128;
  const char* DB = (const char*)a.dim_ + (size_t)(q * 64 + tfo * 16) * (D * 2) + h * 128;
  const unsigned dv0 = (lane >> 3) * (D * 2) + swz0, dv1 = (dv0 ^ 64) + 7 * (D * 2);
  const int pm = p < 28 ? p : 27;
  u32x4 mg, rtv[3];
#define RW(ks, j) _Pragma("unroll") for (int t = 0; t < 4; ++t) wf##ks[t] = *(LAS u32x4*)(sm + wra[((j) >> 2) & 1][t][ks] + ((j) & 3) * 16384)
#define MM(j, ks, df, t) do { if ((j) + (ks)) mfma_acc(acc[df][t], xr[(j) % XRING][(ks) * 2 + (df)], wf##ks[t]); \
                              else mfma_init(acc[df][t], xr[(j) % XRING][(ks) * 2 + (df)], wf##ks[t]); } while (0)
#define MR(ks) mfma_acc(rsa, ones, wf##ks[0])
  u32x4 rsf;
#define RSR(j, ks) rsf = *(LAS u32x4*)(sm + wra[((j) >> 2) & 1][0][ks] + ((j) & 3) * 16384)
#define RSM() mfma_acc(rsa, ones, rsf)
#define PRETOUCH() do { const u32x4 z = {0u, 0u, 0u, ~tag}; \
    const unsigned o = w == 0 ? OFF_STG + (p * 8 + q) * 1024 + lane * 16 : \
                       (lane < 8 ? OFF_F2 + (p * 8 + q) * 128 : (lane < 16 ? OFF_CAND + (q * 32 + p) * 128 - 128 : \
                       (lane < 32 ? OFF_HID + ((lane >> 2) & 3) * 16384 + (p * 8 + q) * 64 - (lane & 28) * 16 : \
                        OFF_ROWH + p * 4096 + ((lane >> 3) & 3) * 1024 + q * 128 - (lane & 56) * 16))) + lane * 16; \
    if (w < 2) gst16(z, ws, o); } while (0)
  float vv[8], v1p, v0p;
#define VCALC() do { v1p = 0; v0p = 0; _Pragma("unroll") for (int u = 0; u < 2; ++u) { \
      const f32x4 sbv = *(LAS f32x4*)(sm + L_MOD + (320 + ech * 8 + u * 4) * 4); \
      const f32x4 cb = *(LAS f32x4*)(sm + L_MOD + (384 + ech * 8 + u * 4) * 4); \
      _Pragma("unroll") for (int i = 0; i < 4; ++i) { \
        const unsigned ww = wre[u * 2 + (i >> 1)]; \
        const float wv = (i & 1) ? bfhi(ww) : bflo(ww); \
        vv[u * 4 + i] = cb[i] * wv; v1p += vv[u * 4 + i]; v0p += sbv[i] * wv; } } \
    v1p = sum8lanes(v1p); v0p = sum8lanes(v0p); TIE2(v1p, v0p); \
    TIE4(vv[0], vv[1], vv[2], vv[3]); TIE4(vv[4], vv[5], vv[6], vv[7]); } while (0)
  f32x4 sa[2], ca[2];
  const unsigned exo = L_SLOT + 5 * 16384 + w * 4096 + lane * 16;
#define EXPUT() do { MFMA_DONE2(acc[0][2], acc[0][3]); TIE2(acc[1][2], acc[1][3]); \
    _Pragma("unroll") for (int df = 0; df < 2; ++df) _Pragma("unroll") for (int u = 0; u < 2; ++u) \
      *(LAS f32x4*)(sm + exo + (df * 2 + u) * 1024) = acc[df][2 + u]; } while (0)
#define RSPUT() do { asm volatile("s_nop 7" : "+v"(rsa)); if (lane < 16) *(LAS float*)(sm + L_RS + (tfo * 16 + lane) * 8 + h * 4) = rsa[0]; } while (0)
  f32x4 rsv[2][2];
#define EPRE() do { _Pragma("unroll") for (int u = 0; u < 2; ++u) { \
      sa[u] = *(LAS f32x4*)(sm + L_MOD + (dq * 32 + u * 16 + fq * 4) * 4); \
      ca[u] = *(LAS f32x4*)(sm + L_MOD + (128 + dq * 32 + u * 16 + fq * 4) * 4); } \
    _Pragma("unroll") for (int u = 0; u < 2; ++u) { const f32x2 r2 = *(LAS f32x2*)(sm + L_RS + ((u ^ tfo) * 16 + fr) * 8); \
      rsv[0][u] = sa[0] * (r2[0] + r2[1]); rsv[1][u] = sa[1] * (r2[0] + r2[1]); } \
    TIE4(rsv[0][0], rsv[0][1], rsv[1][0], rsv[1][1]); TIE2(ca[0], ca[1]); } while (0)
#define GD(kp, sl) glds2<(sl) * 16384, ((kp) & 3) * 256>((kp) < 4 ? DA : DB, dv0, dv1, ldst)
#define LM() gld16<0>(mg, ws + (OFF_MU + (w < 4 ? (w >> 1) * 8 + nt * 2 + (w & 1) : (w - 2) * 8 + q) * 1024), lane * 16)
#define MODOK() __all(__builtin_amdgcn_perm(__builtin_amdgcn_perm(mg[3], mg[2], 0x0c0c0400u), __builtin_amdgcn_perm(mg[1], mg[0], 0x0c0c0400u), 0x05040100u) == tag)
#define MODPUT() do { TIE1(mg); \
    if (__builtin_expect(!MODOK(), 0)) { unsigned sp = 0; \
      do { __builtin_amdgcn_s_sleep(1); LM(); WAITV1(0, mg); } while (!MODOK() && ++sp < SPIN_CAP); } \
    float mv = rbf(tr16(tr32(bitsf(mg[0] & ~0xffu), bitsf(mg[2] & ~0xffu)), tr32(bitsf(mg[1] & ~0xffu), bitsf(mg[3] & ~0xffu)))); \
    if (w == 2 || w == 3 || w == 6) mv = rbf(1.f + mv); \
    if (w < 4 && cc) mv = -mv; \
    *(LAS float*)(sm + L_MOD + w * 256 + fr * 16 + fq * 4) = mv; } while (0)
#define RTS(m) ((m) < 2 ? L_SLOT + 8 * 16384 + (m) * 8192 : L_TR)
#define RT_IN(m) glds<0>((m) == 0 ? a.wg : ((m) == 1 ? a.wu : a.wd), \
      ((m) < 2 ? (q * 64 + et) * (F * 2) + pm * 128 : (pm * 64 + et) * (D * 2) + q * 128) + (ech ^ (((w & 1) * 4 + fq))) * 16, lds0 + RTS(m) + w * 1024)
#define RT_RD() do { _Pragma("unroll") for (int s = 0; s < 2; ++s) { \
        const int r0 = s * 32 + fq * 8 + (fr >> 2), r1 = r0 + 4; \
        const unsigned im = RTS(0) + ((fr >> 1) & 1) * 8192 + (fr & 1) * 8; \
        const u32x2 a0 = __builtin_bit_cast(u32x2, __builtin_amdgcn_ds_read_tr16_b64_v4i16((LAS s16x4*)(sm + im + r0 * 128 + ((w ^ (r0 >> 1)) & 7) * 16))); \
        const u32x2 a1 = __builtin_bit_cast(u32x2, __builtin_amdgcn_ds_read_tr16_b64_v4i16((LAS s16x4*)(sm + im + r1 * 128 + ((w ^ (r1 >> 1)) & 7) * 16))); \
        rtv[s] = u32x4{a0[0], a0[1], a1[0], a1[1]}; } \
      const int rr = fr * 4 + fq; \
      rtv[2] = *(LAS u32x4*)(sm + RTS(2) + rr * 128 + ((w ^ (rr >> 1)) & 7) * 16); TIE2(rtv[0], rtv[1]); TIE1(rtv[2]); } while (0)
#define RT_ST(m) do { if ((m) < 2) gst16(rtv[m], ws + (OFF_RT + ((p * 8 + w) * 16 + q * 2 + (m)) * 1024), lane * 16); \
      else gst16(rtv[2], ws + (OFF_RD + (q * 8 + w) * 32768 + p * 256), fq * 8192 + fr * 16); } while (0)
  RW(0, 0);
  RW(1, 0);
  SCHED();
  WAITV1(7, xr[0][0]);
  MM(0, 0, 0, 0);
  LX(2, 0);
  MM(0, 0, 0, 1);
  MM(0, 0, 0, 2);
  MM(0, 0, 0, 3);
  WAITV1(7, xr[0][1]);
  MM(0, 0, 1, 0);
  LX(2, 1);
  MM(0, 0, 1, 1);
  MR(0);
  MM(0, 0, 1, 2);
  MM(0, 0, 1, 3);
  SCHED();
  RW(0, 1);
  SCHED();
  WAITV1(7, xr[0][2]);
  MM(0, 1, 0, 0);
  LX(2, 2);
  MM(0, 1, 0, 1);
  MM(0, 1, 0, 2);
  MM(0, 1, 0, 3);
  WAITV1(7, xr[0][3]);
  MM(0, 1, 1, 0);
  LX(2, 3);
  MM(0, 1, 1, 1);
  MR(1);
  MM(0, 1, 1, 2);
  MM(0, 1, 1, 3);
  SCHED();
  RW(1, 1);
  SCHED();
  WAITV1(7, xr[1][0]);
  MM(1, 0, 0, 0);
  LX(3, 0);
  MM(1, 0, 0, 1);
  MM(1, 0, 0, 2);
  MM(1, 0, 0, 3);
  WAITV1(7, xr[1][1]);
  MM(1, 0, 1, 0);
  LX(3, 1);
  MM(1, 0, 1, 1);
  MR(0);
  MM(1, 0, 1, 2);
  MM(1, 0, 1, 3);
  SCHED();
  BAR();
  RW(0, 2);
  SCHED();
  WAITV1(7, xr[1][2]);
  MM(1, 1, 0, 0);
  GW(8);
  MM(1, 1, 0, 1);
  MM(1, 1, 0, 2);
  GW(9);
  MM(1, 1, 0, 3);
  WAITV1(10, xr[1][3]);
  MM(1, 1, 1, 0);
  LX(3, 2);
  MM(1, 1, 1, 1);
  MR(1);
  MM(1, 1, 1, 2);
  LX(3, 3);
  MM(1, 1, 1, 3);
  SCHED();
  RW(1, 2);
  SCHED();
  WAITV1(11, xr[2][0]);
  MM(2, 0, 0, 0);
  LX(4, 0);
  MM(2, 0, 0, 1);
  MM(2, 0, 0, 2);
  MM(2, 0, 0, 3);
  WAITV1(11, xr[2][1]);
  MM(2, 0, 1, 0);
  LX(4, 1);
  MM(2, 0, 1, 1);
  MR(0);
  MM(2, 0, 1, 2);
  MM(2, 0, 1, 3);
  SCHED();
  RW(0, 3);
  SCHED();
  WAITV1(11, xr[2][2]);
  MM(2, 1, 0, 0);
  LX(4, 2);
  MM(2, 1, 0, 1);
  MM(2, 1, 0, 2);
  MM(2, 1, 0, 3);
  WAITV1(11, xr[2][3]);
  MM(2, 1, 1, 0);
  LX(4, 3);
  MM(2, 1, 1, 1);
  MR(1);
  MM(2, 1, 1, 2);
  MM(2, 1, 1, 3);
  SCHED();
  RW(1, 3);
  SCHED();
  WAITV1(11, xr[0][0]);
  MM(3, 0, 0, 0);
  LX(5, 0);
  MM(3, 0, 0, 1);
  MM(3, 0, 0, 2);
  MM(3, 0, 0, 3);
  WAITV1(11, xr[0][1]);
  MM(3, 0, 1, 0);
  LX(5, 1);
  MM(3, 0, 1, 1);
  MR(0);
  MM(3, 0, 1, 2);
  MM(3, 0, 1, 3);
  SCHED();
  BAR();
  RW(0, 4);
  SCHED();
  WAITV1(7, xr[0][2]);
  MM(3, 1, 0, 0);
  GW(10);
  MM(3, 1, 0, 1);
  MM(3, 1, 0, 2);
  GW(11);
  MM(3, 1, 0, 3);
  WAITV1(10, xr[0][3]);
  MM(3, 1, 1, 0);
  LX(5, 2);
  MM(3, 1, 1, 1);
  MR(1);
  MM(3, 1, 1, 2);
  LX(5, 3);
  MM(3, 1, 1, 3);
  SCHED();
  RW(1, 4);
  SCHED();
  WAITV1(11, xr[1][0]);
  MM(4, 0, 0, 0);
  LX(6, 0);
  MM(4, 0, 0, 1);
  RSR(8, 0);
  MM(4, 0, 0, 2);
  MM(4, 0, 0, 3);
  WAITV1(11, xr[1][1]);
  MM(4, 0, 1, 0);
  LX(6, 1);
  MM(4, 0, 1, 1);
  MR(0);
  MM(4, 0, 1, 2);
  RSM();
  MM(4, 0, 1, 3);
  SCHED();
  RW(0, 5);
  SCHED();
  WAITV1(11, xr[1][2]);
  MM(4, 1, 0, 0);
  LX(6, 2);
  MM(4, 1, 0, 1);
  RSR(8, 1);
  MM(4, 1, 0, 2);
  MM(4, 1, 0, 3);
  WAITV1(11, xr[1][3]);
  MM(4, 1, 1, 0);
  LX(6, 3);
  MM(4, 1, 1, 1);
  MR(1);
  MM(4, 1, 1, 2);
  RSM();
  MM(4, 1, 1, 3);
  SCHED();
  RW(1, 5);
  SCHED();
  WAITV1(11, xr[2][0]);
  MM(5, 0, 0, 0);
  LX(7, 0);
  MM(5, 0, 0, 1);
  RSR(9, 0);
  MM(5, 0, 0, 2);
  MM(5, 0, 0, 3);
  WAITV1(11, xr[2][1]);
  MM(5, 0, 1, 0);
  LX(7, 1);
  MM(5, 0, 1, 1);
  MR(0);
  MM(5, 0, 1, 2);
  RSM();
  MM(5, 0, 1, 3);
  SCHED();
  BAR();
  PRETOUCH();
  RW(0, 6);
  SCHED();
  WAITV1(7, xr[2][2]);
  MM(5, 1, 0, 0);
  GW(12);
  MM(5, 1, 0, 1);
  RSR(9, 1);
  MM(5, 1, 0, 2);
  GW(13);
  MM(5, 1, 0, 3);
  WAITV1(10, xr[2][3]);
  MM(5, 1, 1, 0);
  LX(7, 2);
  MM(5, 1, 1, 1);
  MR(1);
  MM(5, 1, 1, 2);
  RSM();
  LX(7, 3);
  MM(5, 1, 1, 3);
  SCHED();
  RW(1, 6);
  SCHED();
  WAITV1(11, xr[0][0]);
  MM(6, 0, 0, 0);
  LX(8, 0);
  MM(6, 0, 0, 1);
  RSR(10, 0);
  MM(6, 0, 0, 2);
  LX(8, 1);
  MM(6, 0, 0, 3);
  WAITV1(12, xr[0][1]);
  MM(6, 0, 1, 0);
  MM(6, 0, 1, 1);
  MR(0);
  LM();
  MM(6, 0, 1, 2);
  RSM();
  MM(6, 0, 1, 3);
  SCHED();
  RW(0, 7);
  SCHED();
  WAITV1(12, xr[0][2]);
  MM(6, 1, 0, 0);
  LX(8, 2);
  MM(6, 1, 0, 1);
  RSR(10, 1);
  MM(6, 1, 0, 2);
  MM(6, 1, 0, 3);
  WAITV1(12, xr[0][3]);
  MM(6, 1, 1, 0);
  LX(8, 3);
  MM(6, 1, 1, 1);
  MR(1);
  MM(6, 1, 1, 2);
  RSM();
  MM(6, 1, 1, 3);
  SCHED();
  RW(1, 7);
  SCHED();
  WAITV1(12, xr[1][0]);
  MM(7, 0, 0, 0);
  LX(9, 0);
  MM(7, 0, 0, 1);
  RSR(11, 0);
  MM(7, 0, 0, 2);
  MM(7, 0, 0, 3);
  WAITV1(12, xr[1][1]);
  MM(7, 0, 1, 0);
  LX(9, 1);
  MM(7, 0, 1, 1);
  MR(0);
  MM(7, 0, 1, 2);
  RSM();
  MM(7, 0, 1, 3);
  SCHED();
  BAR();
  RW(0, 8);
  SCHED();
  WAITV1(8, xr[1][2]);
  MM(7, 1, 0, 0);
  GW(14);
  MM(7, 1, 0, 1);
  RSR(11, 1);
  MM(7, 1, 0, 2);
  GW(15);
  MM(7, 1, 0, 3);
  WAITV1(11, xr[1][3]);
  MM(7, 1, 1, 0);
  LX(9, 2);
  MM(7, 1, 1, 1);
  MR(1);
  MM(7, 1, 1, 2);
  RSM();
  LX(9, 3);
  MM(7, 1, 1, 3);
  SCHED();
  RW(1, 8);
  SCHED();
  WAITV1(12, xr[2][0]);
  MM(8, 0, 0, 0);
  LX(10, 0);
  MM(8, 0, 0, 1);
  RSR(12, 0);
  MM(8, 0, 0, 2);
  MM(8, 0, 0, 3);
  WAITV1(12, xr[2][1]);
  MM(8, 0, 1, 0);
  LX(10, 1);
  MM(8, 0, 1, 1);
  MM(8, 0, 1, 2);
  RSM();
  MM(8, 0, 1, 3);
  SCHED();
  RW(0, 9);
  SCHED();
  WAITV1(11, xr[2][2]);
  MM(8, 1, 0, 0);
  LX(10, 2);
  MM(8, 1, 0, 1);
  RSR(12, 1);
  MM(8, 1, 0, 2);
  MM(8, 1, 0, 3);
  WAITV1(11, xr[2][3]);
  MM(8, 1, 1, 0);
  LX(10, 3);
  MM(8, 1, 1, 1);
  MM(8, 1, 1, 2);
  RSM();
  MM(8, 1, 1, 3);
  SCHED();
  RW(1, 9);
  SCHED();
  WAITV1(11, xr[0][0]);
  MM(9, 0, 0, 0);
  LX(11, 0);
  MM(9, 0, 0, 1);
  RSR(13, 0);
  MM(9, 0, 0, 2);
  MM(9, 0, 0, 3);
  WAITV1(11, xr[0][1]);
  MM(9, 0, 1, 0);
  LX(11, 1);
  MM(9, 0, 1, 1);
  MM(9, 0, 1, 2);
  RSM();
  MM(9, 0, 1, 3);
  SCHED();
  WAITV(16);
  MODPUT();
  BAR();
  RW(0, 10);
  SCHED();
  WAITV1(7, xr[0][2]);
  MM(9, 1, 0, 0);
  LX(11, 2);
  MM(9, 1, 0, 1);
  RSR(13, 1);
  MM(9, 1, 0, 2);
  MM(9, 1, 0, 3);
  WAITV1(7, xr[0][3]);
  MM(9, 1, 1, 0);
  LX(11, 3);
  MM(9, 1, 1, 1);
  MM(9, 1, 1, 2);
  RSM();
  MM(9, 1, 1, 3);
  SCHED();
  RW(1, 10);
  SCHED();
  WAITV1(7, xr[1][0]);
  MM(10, 0, 0, 0);
  LX(12, 0);
  MM(10, 0, 0, 1);
  RSR(14, 0);
  MM(10, 0, 0, 2);
  MM(10, 0, 0, 3);
  WAITV1(7, xr[1][1]);
  MM(10, 0, 1, 0);
  LX(12, 1);
  MM(10, 0, 1, 1);
  MM(10, 0, 1, 2);
  RSM();
  MM(10, 0, 1, 3);
  SCHED();
  RW(0, 11);
  SCHED();
  WAITV1(7, xr[1][2]);
  MM(10, 1, 0, 0);
  LX(12, 2);
  MM(10, 1, 0, 1);
  RSR(14, 1);
  MM(10, 1, 0, 2);
  MM(10, 1, 0, 3);
  WAITV1(7, xr[1][3]);
  MM(10, 1, 1, 0);
  LX(12, 3);
  MM(10, 1, 1, 1);
  MM(10, 1, 1, 2);
  RSM();
  MM(10, 1, 1, 3);
  SCHED();
  RW(1, 11);
  SCHED();
  WAITV1(7, xr[2][0]);
  MM(11, 0, 0, 0);
  LX(13, 0);
  MM(11, 0, 0, 1);
  RSR(15, 0);
  MM(11, 0, 0, 2);
  MM(11, 0, 0, 3);
  WAITV1(7, xr[2][1]);
  MM(11, 0, 1, 0);
  LX(13, 1);
  MM(11, 0, 1, 1);
  MM(11, 0, 1, 2);
  RSM();
  MM(11, 0, 1, 3);
  SCHED();
  BAR();
  VCALC();
  RW(0, 12);
  SCHED();
  WAITV1(7, xr[2][2]);
  MM(11, 1, 0, 0);
  LX(13, 2);
  MM(11, 1, 0, 1);
  RSR(15, 1);
  MM(11, 1, 0, 2);
  MM(11, 1, 0, 3);
  WAITV1(7, xr[2][3]);
  MM(11, 1, 1, 0);
  LX(13, 3);
  MM(11, 1, 1, 1);
  MM(11, 1, 1, 2);
  RSM();
  MM(11, 1, 1, 3);
  SCHED();
  RW(1, 12);
  SCHED();
  WAITV1(7, xr[0][0]);
  MM(12, 0, 0, 0);
  LX(14, 0);
  MM(12, 0, 0, 1);
  MM(12, 0, 0, 2);
  MM(12, 0, 0, 3);
  WAITV1(7, xr[0][1]);
  MM(12, 0, 1, 0);
  LX(14, 1);
  MM(12, 0, 1, 1);
  MM(12, 0, 1, 2);
  MM(12, 0, 1, 3);
  SCHED();
  RW(0, 13);
  SCHED();
  WAITV1(7, xr[0][2]);
  MM(12, 1, 0, 0);
  LX(14, 2);
  MM(12, 1, 0, 1);
  MM(12, 1, 0, 2);
  MM(12, 1, 0, 3);
  WAITV1(7, xr[0][3]);
  MM(12, 1, 1, 0);
  LX(14, 3);
  MM(12, 1, 1, 1);
  MM(12, 1, 1, 2);
  MM(12, 1, 1, 3);
  SCHED();
  RW(1, 13);
  SCHED();
  WAITV1(7, xr[1][0]);
  MM(13, 0, 0, 0);
  LX(15, 0);
  MM(13, 0, 0, 1);
  MM(13, 0, 0, 2);
  MM(13, 0, 0, 3);
  WAITV1(7, xr[1][1]);
  MM(13, 0, 1, 0);
  LX(15, 1);
  MM(13, 0, 1, 1);
  MM(13, 0, 1, 2);
  MM(13, 0, 1, 3);
  SCHED();
  RSPUT();
  BAR();
  RW(0, 14);
  SCHED();
  LX(15, 2);
  WAITV1(8, xr[1][2]);
  MM(13, 1, 0, 0);
  LX(15, 3);
  MM(13, 1, 0, 1);
  MM(13, 1, 0, 2);
  RT_IN(0);
  MM(13, 1, 0, 3);
  RT_IN(1);
  WAITV1(10, xr[1][3]);
  MM(13, 1, 1, 0);
  MM(13, 1, 1, 1);
  RT_IN(2);
  MM(13, 1, 1, 2);
  MM(13, 1, 1, 3);
  SCHED();
  RW(1, 14);
  SCHED();
  WAITV1(10, xr[2][0]);
  MM(14, 0, 0, 0);
  GD(0, 0);
  MM(14, 0, 0, 1);
  MM(14, 0, 0, 2);
  MM(14, 0, 0, 3);
  WAITV1(11, xr[2][1]);
  MM(14, 0, 1, 0);
  MM(14, 0, 1, 1);
  MM(14, 0, 1, 2);
  MM(14, 0, 1, 3);
  SCHED();
  BAR();
  EPRE();
  RW(0, 15);
  SCHED();
  WAITV1(10, xr[2][2]);
  MM(14, 1, 0, 0);
  GD(4, 1);
  MM(14, 1, 0, 1);
  MM(14, 1, 0, 2);
  MM(14, 1, 0, 3);
  WAITV1(11, xr[2][3]);
  MM(14, 1, 1, 0);
  MM(14, 1, 1, 1);
  MM(14, 1, 1, 2);
  MM(14, 1, 1, 3);
  SCHED();
  RW(1, 15);
  SCHED();
  WAITV(9); TIE2(xr[0][0], xr[0][1]);
  MM(15, 0, 0, 2);
  MM(15, 0, 0, 3);
  MM(15, 0, 1, 2);
  MM(15, 0, 1, 3);
  GD(1, 2);
  WAITV(9); TIE2(xr[0][2], xr[0][3]);
  MM(15, 1, 0, 2);
  MM(15, 1, 0, 3);
  MM(15, 1, 1, 2);
  MM(15, 1, 1, 3);
  MM(15, 0, 0, 0); MM(15, 0, 0, 1);
  SCHED();
  EXPUT();
  SCHED();
  SCHED();
  SCHED();
  MM(15, 0, 1, 0); MM(15, 0, 1, 1);
  GD(5, 3);
  MM(15, 1, 0, 0); MM(15, 1, 0, 1); MM(15, 1, 1, 0); MM(15, 1, 1, 1);
  SCHED();
  STAMP(4);
  {
    BAR();
    f32x4 o[2][2];
#pragma unroll
    for (int df = 0; df < 2; ++df)
#pragma unroll
      for (int u = 0; u < 2; ++u) o[df][u] = *(LAS f32x4*)(sm + (exo ^ 16384) + (df * 2 + u) * 1024);
    MFMA_DONE2(acc[0][0], acc[0][1]); TIE2(acc[1][0], acc[1][1]);
#pragma unroll
    for (int df = 0; df < 2; ++df) {
      unsigned pk[2][2];
#pragma unroll
      for (int u = 0; u < 2; ++u) {
        const f32x4 v = (acc[df][u] + o[df][u]) * ca[df] + rsv[df][u];
        pk[u][0] = pk2(v[0], v[1]); pk[u][1] = pk2(v[2], v[3]);
      }
      auto r0 = __builtin_amdgcn_permlane16_swap(pk[0][0], pk[1][0], false, false);
      auto r1 = __builtin_amdgcn_permlane16_swap(pk[0][1], pk[1][1], false, false);
      gst16(u32x4{r0[0], r1[0], r0[1], r1[1]}, ws + (OFF_PB + p * 4 * 32768 + (cc * 16 + nt * 4 + dq) * 1024 + df * 512),
            ((fq & 1) ^ tfo) * 32768 + ((fq >> 1) * 16 + fr) * 16);
    }
  }
  RAWBAR();
  GD(2, 4);
  WAITV(WD_MID_N);
  RAWBAR();
  STAMP(5);
  if (tid < 8) gst16(u32x4{fbits(v1p), fbits(v0p), 0u, tag}, ws + (OFF_F2 + (p * 8 + q) * 128), tid * 16);
  RAWBAR();
  u32x4 xe;
#define LXE() pld16(xe, a.x, (p * 64 + et) * (D * 2) + q * 128 + ech * 16)
  f32x4 ac2[4];
  {
    u32x4 pf[16];
    const char* PA = ws + OFF_PB + (p * 4 + dq) * 32768 + h * 16384;
    unsigned sb[4][2];
#pragma unroll
    for (int i = 0; i < 4; ++i) {
      sb[i][0] = fro + (h ? DSLOT1(i) : DSLOT0(i)) * 16384; sb[i][1] = sb[i][0] ^ 64;
      TIE2(sb[i][0], sb[i][1]);
    }
    u32x4 df_[DRING];
#define LP(s) gld16<((s) & 3) * 1024>(pf[s], PA + ((s) & 12) * 1024, lane * 16)
#define RD(n) df_[(n) % DRING] = *(LAS u32x4*)(sm + sb[(n) >> 4][((n) >> 2) & 1] + (((n) >> 3) & 1) * 8192 + ((n) & 3) * 2048)
#define M2(n) do { if ((n) >= 4) mfma_acc(ac2[(n) & 3], df_[(n) % DRING], pf[(n) >> 2]); else mfma_init(ac2[(n) & 3], df_[(n) % DRING], pf[(n) >> 2]); } while (0)
  RT_RD();
  GD(6, 7);
  GD(3, 5);
  LXE();
  RD(0);
  RD(1);
  RD(2);
  RD(3);
  RD(4);
  RD(5);
  RD(6);
  RD(7);
  RD(8);
  RD(9);
  RD(10);
  RD(11);
  RD(12);
  RD(13);
  RD(14);
  RD(15);
  RD(16);
  RD(17);
  RD(18);
  RD(19);
  RD(20);
  RD(21);
  RD(22);
  RD(23);
  if (w == 1) {
    __builtin_amdgcn_s_sleep(DLY2);
    u32x4 g;
    poll16(g, ws + OFF_F2 + p * 1024, ech * 128, tag);
    const float V1 = sum8lanes(bitsf(g[0])), V0 = sum8lanes(bitsf(g[1]));
    if (lane == 0) *(LAS f32x2*)(sm + L_V) = f32x2{V1, V0};
  }
  WAITV(WD_LATE_N);
  RAWBAR();
  STAMP(6);
  GD(7, 8);
  LP(0);
  LP(1);
  LP(2);
  LP(3);
  LP(4);
  SCHED();
  RD(24);
  WAITV1(4, pf[0]);
  SCHED();
  M2(0);
  SCHED();
  LP(5);
  SCHED();
  M2(1);
  SCHED();
  RD(25);
  SCHED();
  M2(2);
  SCHED();
  RD(26);
  SCHED();
  M2(3);
  SCHED();
  RD(27);
  WAITV1(4, pf[1]);
  SCHED();
  M2(4);
  SCHED();
  LP(6);
  SCHED();
  M2(5);
  SCHED();
  RD(28);
  SCHED();
  M2(6);
  SCHED();
  RD(29);
  SCHED();
  M2(7);
  SCHED();
  RD(30);
  WAITV1(4, pf[2]);
  SCHED();
  M2(8);
  SCHED();
  LP(7);
  SCHED();
  M2(9);
  SCHED();
  RD(31);
  SCHED();
  M2(10);
  SCHED();
  RD(32);
  SCHED();
  M2(11);
  SCHED();
  RD(33);
  WAITV1(4, pf[3]);
  SCHED();
  M2(12);
  SCHED();
  RD(34);
  LP(8);
  SCHED();
  M2(13);
  SCHED();
  SCHED();
  M2(14);
  SCHED();
  RD(35);
  SCHED();
  M2(15);
  SCHED();
  RD(36);
  WAITV1(4, pf[4]);
  SCHED();
  M2(16);
  SCHED();
  RD(37);
  LP(9);
  SCHED();
  M2(17);
  SCHED();
  SCHED();
  M2(18);
  SCHED();
  RD(38);
  SCHED();
  M2(19);
  SCHED();
  RD(39);
  WAITV1(4, pf[5]);
  SCHED();
  M2(20);
  SCHED();
  RD(40);
  LP(10);
  SCHED();
  M2(21);
  SCHED();
  SCHED();
  M2(22);
  SCHED();
  RD(41);
  SCHED();
  M2(23);
  SCHED();
  RD(42);
  WAITV1(4, pf[6]);
  SCHED();
  M2(24);
  SCHED();
  RD(43);
  LP(11);
  SCHED();
  M2(25);
  SCHED();
  RD(44);
  SCHED();
  M2(26);
  SCHED();
  SCHED();
  M2(27);
  SCHED();
  RD(45);
  WAITV1(4, pf[7]);
  SCHED();
  M2(28);
  SCHED();
  RD(46);
  LP(12);
  SCHED();
  M2(29);
  SCHED();
  RD(47);
  SCHED();
  M2(30);
  SCHED();
  SCHED();
  M2(31);
  SCHED();
  WAITV1(4, pf[8]);
  SCHED();
  M2(32);
  SCHED();
  LP(13);
  SCHED();
  M2(33);
  SCHED();
  SCHED();
  M2(34);
  SCHED();
  SCHED();
  M2(35);
  SCHED();
  RAWBAR();
  RD(48);
  RD(49);
  RD(50);
  RD(51);
  WAITV1(4, pf[9]);
  SCHED();
  M2(36);
  SCHED();
  RD(52);
  LP(14);
  SCHED();
  M2(37);
  SCHED();
  RD(53);
  SCHED();
  M2(38);
  SCHED();
  RD(54);
  SCHED();
  M2(39);
  SCHED();
  WAITV1(4, pf[10]);
  SCHED();
  M2(40);
  SCHED();
  RD(55);
  LP(15);
  SCHED();
  M2(41);
  SCHED();
  RD(56);
  SCHED();
  M2(42);
  SCHED();
  RD(57);
  SCHED();
  M2(43);
  SCHED();
  WAITV1(4, pf[11]);
  SCHED();
  M2(44);
  SCHED();
  RD(58);
  RT_ST(0);
  SCHED();
  M2(45);
  SCHED();
  RD(59);
  SCHED();
  M2(46);
  SCHED();
  RD(60);
  SCHED();
  M2(47);
  SCHED();
  WAITV1(4, pf[12]);
  SCHED();
  M2(48);
  SCHED();
  RD(61);
  RT_ST(1);
  SCHED();
  M2(49);
  SCHED();
  RD(62);
  SCHED();
  M2(50);
  SCHED();
  RD(63);
  SCHED();
  M2(51);
  SCHED();
  WAITV1(4, pf[13]);
  SCHED();
  M2(52);
  SCHED();
  RT_ST(2);
  SCHED();
  M2(53);
  SCHED();
  SCHED();
  M2(54);
  SCHED();
  SCHED();
  M2(55);
  SCHED();
  WAITV1(4, pf[14]);
  SCHED();
  M2(56);
  SCHED();
  SCHED();
  M2(57);
  SCHED();
  SCHED();
  M2(58);
  SCHED();
  SCHED();
  M2(59);
  SCHED();
  WAITV1(3, pf[15]);
  SCHED();
  M2(60);
  SCHED();
  SCHED();
  M2(61);
  SCHED();
  SCHED();
  M2(62);
  SCHED();
  SCHED();
  M2(63);
  SCHED();
  }
  STAMP(7);
  constexpr int RSB = 272;
  float x1[8];
  {
    f32x4 ga[2];
#pragma unroll
    for (int u = 0; u < 2; ++u) ga[u] = *(LAS f32x4*)(sm + L_MOD + (256 + ech * 8 + u * 4) * 4);
    MFMA_DONE2(ac2[0], ac2[1]); TIE2(ac2[2], ac2[3]);
#pragma unroll
    for (int e = 0; e < 4; ++e) *(LAS f32x4*)(sm + L_SLOT + YSLOT * 16384 + (h * 64 + dq * 16 + fr) * RSB + fq * 16 + e * 64) = ac2[e];
    WAITV(0); TIE1(xe);
    BAR();
    float s0 = 0, s1 = 0, s2 = 0;
#pragma unroll
    for (int u = 0; u < 2; ++u) {
      const unsigned yo = L_SLOT + YSLOT * 16384 + et * RSB + ech * 32;
      const f32x4 y = *(LAS f32x4*)(sm + yo + u * 16) + *(LAS f32x4*)(sm + yo + 64 * RSB + u * 16);
#pragma unroll
      for (int i = 0; i < 2; ++i) {
        const unsigned xw = xe[u * 2 + i];
        const unsigned pk = pk2(bflo(xw) + ga[u][2 * i] * y[2 * i], bfhi(xw) + ga[u][2 * i + 1] * y[2 * i + 1]);
        xe[u * 2 + i] = pk;
        s0 = dot2(pk, 0x3f803f80u, s0); s1 = dot2(pk, pk, s1);
        x1[u * 4 + 2 * i] = bflo(pk); x1[u * 4 + 2 * i + 1] = bfhi(pk);
      }
    }
#pragma unroll
    for (int i = 0; i < 8; ++i) s2 += x1[i] * vv[i];
    s0 = sum8lanes(s0); s1 = sum8lanes(s1); s2 = sum8lanes(s2);
    if (ech == 0) gst16(u32x4{fbits(s0), fbits(s1), fbits(s2), tag}, ws + (OFF_STG + (p * 8 + q) * 1024), et * 16);
  }
  STAMP(8);
  u32x4 outv, bfr[2];
  {
    f32x4 sbv[2], cb[2], gb[2];
#pragma unroll
    for (int u = 0; u < 2; ++u) {
      sbv[u] = *(LAS f32x4*)(sm + L_MOD + (320 + ech * 8 + u * 4) * 4);
      cb[u] = *(LAS f32x4*)(sm + L_MOD + (384 + ech * 8 + u * 4) * 4);
      gb[u] = *(LAS f32x4*)(sm + L_MOD + (448 + ech * 8 + u * 4) * 4);
    }
    const f32x2 V = *(LAS f32x2*)(sm + L_V);
    __builtin_amdgcn_s_sleep(DLYA);
    u32x4 g;
    poll16(g, ws + OFF_STG + (p * 8 + w) * 1024, lane * 16, tag);
    *(LAS u32x4*)(sm + L_TR + tid * 16) = g;
    BAR();
    STAMP(9);
    f32x4 t = *(LAS f32x4*)(sm + L_TR + lane * 16);
#pragma unroll
    for (int i = 1; i < 8; ++i) t += *(LAS f32x4*)(sm + L_TR + i * 1024 + lane * 16);
    const float m = t[0] * (1.f / D);
    const float rstd = __builtin_amdgcn_rsqf(fmaxf(t[1] * (1.f / D) - m * m, 0.f) + EPS);
    const float r = rstd * (t[2] - m * V[0]) + V[1];
    unsigned u = fbits(r);
    u = (u & 0x80000000u) ? ~u : (u | 0x80000000u);
    const unsigned key = (u & ~63u) | (unsigned)(63 - lane);
    const float me = bitsf(__builtin_amdgcn_ds_bpermute(et * 4, fbits(m))), re = bitsf(__builtin_amdgcn_ds_bpermute(et * 4, fbits(rstd)));
    const unsigned k0 = wave_max(key);
    const unsigned k1 = wave_max(key == k0 ? 0u : key);
    if (tid < 8) gst16(u32x4{k0, k1, 0u, tag}, ws + (OFF_CAND + (q * 32 + p) * 128), tid * 16);
    float h2[8];
#pragma unroll
    for (int i = 0; i < 8; ++i) h2[i] = (x1[i] - me) * re * cb[i >> 2][i & 3] + sbv[i >> 2][i & 3];
    const int r0 = 63 - (int)(k0 & 63u), r1 = 63 - (int)(k1 & 63u);
    if (et == r0 || et == r1) {
      const unsigned ro = (et == r0 ? 0 : 2048) + ech * 16;
      gst16(u32x4{pk2(h2[0], h2[1]), pk2(h2[2], h2[3]), 0u, tag}, ws + (OFF_ROWH + p * 4096 + q * 128), ro);
      gst16(u32x4{pk2(h2[4], h2[5]), pk2(h2[6], h2[7]), 0u, tag}, ws + (OFF_ROWH + p * 4096 + q * 128 + 1024), ro);
      gst16(u32x4{xe[0], xe[1], 0u, tag}, ws + (OFF_ROWX + p * 4096 + q * 128), ro);
      gst16(u32x4{xe[2], xe[3], 0u, tag}, ws + (OFF_ROWX + p * 4096 + q * 128 + 1024), ro);
    }
    gld16<0>(bfr[0], ws + (OFF_RT + ((p * 8 + q) * 8 + w) * 2048), lane * 16);
    gld16<1024>(bfr[1], ws + (OFF_RT + ((p * 8 + q) * 8 + w) * 2048), lane * 16);
#pragma unroll
    for (int i = 0; i < 4; ++i)
      outv[i] = pk2(x1[2 * i] + gb[i >> 1][(2 * i) & 3] * h2[2 * i], x1[2 * i + 1] + gb[i >> 1][(2 * i + 1) & 3] * h2[2 * i + 1]);
    TIE1(outv);
  }
  STAMP(10);
  int sel0, sel1;
  unsigned key0, key1;
  {
    if (w == 1) {
      __builtin_amdgcn_s_sleep(DLYB);
      u32x4 g;
      poll16(g, ws + OFF_CAND + xcd * 4096, (lane >> 1) * 128, tag);
      const unsigned kk = (lane & 1) ? g[1] : g[0];
      const unsigned a0 = wave_max(kk);
      const int l0 = __builtin_ctzll(__ballot(kk == a0));
      const unsigned g2 = lane == l0 ? 0u : kk;
      const unsigned a1 = wave_max(g2);
      const int l1 = __builtin_ctzll(__ballot(g2 == a1 && lane != l0));
      if (lane == 0) *(LAS u32x4*)(sm + L_RS) = u32x4{a0, a1, (unsigned)l0, (unsigned)l1};
    }
    BAR();
    const u32x4 e = *(LAS u32x4*)(sm + L_RS);
    key0 = __builtin_amdgcn_readfirstlane(e[0]); key1 = __builtin_amdgcn_readfirstlane(e[1]);
    sel0 = __builtin_amdgcn_readfirstlane(e[2]); sel1 = __builtin_amdgcn_readfirstlane(e[3]);
  }
  STAMP(11);
  const int row0 = (sel0 >> 1) * 64 + 63 - (int)(key0 & 63u), row1 = (sel1 >> 1) * 64 + 63 - (int)(key1 & 63u);
  const int rk = xcd & 1, rcl = (xcd & 2) * 2 + (p & 3), rch = q * 8 + rcl;
  u32x4 hg, wd4[4], fx;
  {
    const unsigned ho = ((fr & 2) ? sel1 : sel0) * 2048 + (fr & 1) * 1024 + (w * 8 + ((fr >> 2) & 1) * 4 + fq) * 16;
    poll16(hg, ws + OFF_ROWH, ho, tag);
    TIE2(bfr[0], bfr[1]);
    if (xcd >= 4 && w < 7) {
      const char* RDp = ws + OFF_RD + rch * 32768;
      gld16<0>(wd4[0], RDp, tid * 16); gld16<0>(wd4[1], RDp + 8192, tid * 16);
      gld16<0>(wd4[2], RDp + 16384, tid * 16); gld16<0>(wd4[3], RDp + 24576, tid * 16);
    }
  }
  STAMP(12);
  if (p < 28) {
    const bool u1 = fr & 1, s1 = (fr >> 2) & 1;
    const unsigned lo0 = u1 ? 0u : hg[0], lo1 = u1 ? 0u : hg[1], hi0 = u1 ? hg[0] : 0u, hi1 = u1 ? hg[1] : 0u;
    const u32x4 a0 = s1 ? u32x4{0, 0, 0, 0} : u32x4{lo0, lo1, hi0, hi1};
    const u32x4 a1 = s1 ? u32x4{lo0, lo1, hi0, hi1} : u32x4{0, 0, 0, 0};
    f32x4 z = mfma(a0, bfr[0], f32x4{0, 0, 0, 0});
    z = mfma(a1, bfr[1], z);
    if (lane < 32) {
      *(LAS float*)(sm + L_RED + (fr * 16 + w * 2 + fq) * 4) = z[0] + z[1];
      *(LAS float*)(sm + L_RED + 1024 + (fr * 16 + w * 2 + fq) * 4) = z[2] + z[3];
    }
    BAR();
    if (w == 7) {
      const f32x4 p0 = *(LAS f32x4*)(sm + L_RED + (fq & 1) * 1024 + fr * 64 + (fq >> 1) * 32);
      const f32x4 p1 = *(LAS f32x4*)(sm + L_RED + (fq & 1) * 1024 + fr * 64 + (fq >> 1) * 32 + 16);
      float zz = ((p0[0] + p0[1]) + (p0[2] + p0[3])) + ((p1[0] + p1[1]) + (p1[2] + p1[3]));
      zz = rbf(tr32(zz, zz));
      const float zu = dppf<DPP_ROR8>(zz);
      const float hid = rbf(zz * sigm(zz)) * zu;
      const unsigned hp = pk2(hid, dppf<DPP_X1>(hid));
      const unsigned h01 = __builtin_amdgcn_ds_bpermute((lane & 48) * 4 + (lane & 1) * 16, hp);
      const unsigned h23 = __builtin_amdgcn_ds_bpermute((lane & 48) * 4 + (lane & 1) * 16 + 8, hp);
      if (fr < 4) gst16(u32x4{h01, h23, 0u, tag}, ws + (OFF_HID + (p * 8 + q) * 64), fq * 16384 + fr * 16);
    }
  }
  {
    const int row = p * 64 + et;
    if (row != row0 && row != row1) *(GAS u32x4*)((GAS char*)a.out + (unsigned)(row * (D * 2) + q * 128 + ech * 16)) = outv;
    if (b == 0 && tid == 0) __hip_atomic_store((GAS unsigned*)ws, tag, __ATOMIC_RELAXED, __HIP_MEMORY_SCOPE_AGENT);
  }
  STAMP(13);
  if (xcd >= 4) {
    const unsigned fo = ((lane & 2) ? OFF_ROWX : OFF_ROWH) + (rk ? sel1 : sel0) * 2048 + (lane & 1) * 1024 + rch * 16;
    float hh, xx, sg, gbv;
    if (w == 7) {
      poll16(fx, ws, fo, tag);
      const unsigned kk = rk ? key1 : key0;
      sg = rbf(sigm(rbf(bitsf((kk & 0x80000000u) ? (kk & 0x7fffffffu) : ~kk))));
      const unsigned sw = (lane & 2) ? 1 : 0, src = (lane >> 2) & 1;
      const unsigned h0 = __builtin_amdgcn_ds_bpermute(src * 4, fx[0]), h1 = __builtin_amdgcn_ds_bpermute(src * 4, fx[1]);
      const unsigned x0 = __builtin_amdgcn_ds_bpermute(src * 4 + 8, fx[0]), x1w = __builtin_amdgcn_ds_bpermute(src * 4 + 8, fx[1]);
      const unsigned hw = sw ? h1 : h0, xw = sw ? x1w : x0;
      hh = (lane & 1) ? bfhi(hw) : bflo(hw); xx = (lane & 1) ? bfhi(xw) : bflo(xw);
      gbv = *(LAS float*)(sm + L_MOD + (448 + rcl * 8 + ech) * 4);
      TIE4(hh, xx, sg, gbv);
    } else {
      __builtin_amdgcn_s_sleep(DLYC);
      u32x4 g;
      poll16(g, ws + OFF_HID + (xcd - 4) * 16384, (tid >> 1) * 64 + (tid & 1) * 16, tag);
      TIE4(wd4[0], wd4[1], wd4[2], wd4[3]);
      const float hv[4] = {bflo(g[0]), bfhi(g[0]), bflo(g[1]), bfhi(g[1])};
      float y[8];
#pragma unroll
      for (int i = 0; i < 4; ++i) {
        y[2 * i] = (hv[0] * bflo(wd4[0][i]) + hv[1] * bflo(wd4[1][i])) + (hv[2] * bflo(wd4[2][i]) + hv[3] * bflo(wd4[3][i]));
        y[2 * i + 1] = (hv[0] * bfhi(wd4[0][i]) + hv[1] * bfhi(wd4[1][i])) + (hv[2] * bfhi(wd4[2][i]) + hv[3] * bfhi(wd4[3][i]));
      }
      float t4[4], t2[2];
#pragma unroll
      for (int i = 0; i < 4; ++i) t4[i] = tr32(y[i], y[i + 4]);
#pragma unroll
      for (int i = 0; i < 2; ++i) { t2[i] = tr16(t4[i], t4[i + 2]); t2[i] = sum8lanes(t2[i]); t2[i] += dppf<DPP_M>(t2[i]); }
      if (fr == 0) *(LAS f32x2*)(sm + L_Y + w * 32 + fq * 8) = f32x2{t2[0], t2[1]};
    }
    BAR();
    if (w == 7) {
      float y = lane < 56 ? *(LAS float*)(sm + L_Y + lane * 4) : 0.f;
      y += dppf<DPP_ROR8>(y); y = tr16(y, y); y = tr32(y, y);
      const float o = xx + rbf(gbv * rbf(hh + rbf(rbf(y) * sg)));
      const unsigned o01 = pk2(o, dppf<DPP_X1>(o));
      const unsigned o23 = dppu<DPP_X2>(o01);
      if ((lane & 59) == 0)
        *(GAS u32x2*)((GAS char*)a.out + (unsigned)((rk ? row1 : row0) * (D * 2) + rch * 16 + (lane >> 2) * 8)) = u32x2{o01, o23};
    }
  }
  STAMP(14);
}

extern "C" void kernel_launch(void* const* d_in, const int* in_sizes, int n_in, void* d_out, int out_size, void* d_ws,
                              size_t ws_size, hipStream_t stream) {
  Args a;
  a.x = (const u16*)d_in[0]; a.sre = (const u16*)d_in[1]; a.sim = (const u16*)d_in[2];
  a.dre = (const u16*)d_in[3]; a.dim_ = (const u16*)d_in[4]; a.wr = (const u16*)d_in[6];
  a.wg = (const u16*)d_in[7]; a.wu = (const u16*)d_in[8]; a.wd = (const u16*)d_in[9];
  a.c = (const u16*)d_in[10]; a.wmod = (const u16*)d_in[11];
  a.ws = (char*)d_ws; a.out = (u16*)d_out;
  (void)hipFuncSetAttribute((const void*)k_mega, hipFuncAttributeMaxDynamicSharedMemorySize, LDS_BYTES);
  k_mega<<<256, 512, LDS_BYTES, stream>>>(a);
}
```
